```python
import torch

def _softmax(x, axis=-1):
    return torch.softmax(x, dim=axis)
def _gelu(x):
    return torch.nn.functional.gelu(x, approximate="tanh")
def _silu(x):
    return torch.nn.functional.silu(x)
def _sigmoid(x):
    return torch.sigmoid(x)
def _layernorm(x, axis=-1, eps=1e-5):
    m = x.mean(dim=axis, keepdim=True)
    v = x.var(dim=axis, keepdim=True, unbiased=False)
    return (x - m) / torch.sqrt(v + eps)
def _scatter_add(tgt, idx, src, axis=0):
    return tgt.clone().index_add_(axis, idx, src)
def _segment_sum(src, seg, n):
    out = torch.zeros((n,) + src.shape[1:], dtype=src.dtype)
    return out.index_add_(0, seg, src)
def _pad(x, pw):
    flat = tuple(v for pair in reversed(pw) for v in pair)
    return torch.nn.functional.pad(x, flat)

kernel_name = 'gated_mix__ssm_conv_s5__swiglu_ffn__prompt'

def setup_inputs():

    _w = (lambda a: (lambda t: t.to(torch.bfloat16) if t.is_floating_point() else t)(torch.as_tensor(a)))
    def _A_setup():
        B, S, D = 1, 2048, 640
        P = D
        Lambda_re = -0.5 * (lambda _x: torch.exp(torch.as_tensor(_x)))((lambda *s: torch.randn(*s, dtype=torch.bfloat16) * 0.2)(P))
        Lambda_im = (lambda *s: torch.randn(*s, dtype=torch.bfloat16) * 0.4)(P) * 1.0
        B_re = (lambda *s: torch.randn(*s, dtype=torch.bfloat16) * 0.2)(P, D) * (D ** -0.5)
        B_im = (lambda *s: torch.randn(*s, dtype=torch.bfloat16) * 0.2)(P, D) * (D ** -0.5)
        C_re = (lambda *s: torch.randn(*s, dtype=torch.bfloat16) * 0.2)(D, P) * (P ** -0.5)
        C_im = (lambda *s: torch.randn(*s, dtype=torch.bfloat16) * 0.2)(D, P) * (P ** -0.5)
        log_dt = (lambda *s: torch.randn(*s, dtype=torch.bfloat16) * 0.2)(P) * 0.3 - 2.0
        Dskip = (lambda *s: torch.randn(*s, dtype=torch.bfloat16) * 0.05)(D)
        return {
            "x": _w((lambda *s: torch.randn(*s, dtype=torch.bfloat16) * 0.05)(B, S, D)),
            "Lambda_re": _w(Lambda_re),
            "Lambda_im": _w(Lambda_im),
            "B_re": _w(B_re),
            "B_im": _w(B_im),
            "C_re": _w(C_re),
            "C_im": _w(C_im),
            "log_dt": _w(log_dt),
            "Dskip": _w(Dskip),
        }
    _A_in = _A_setup()

    def _B_setup():
        B, S, D, F = 1, 2048, 640, 1728
        return {
            "x": _w((lambda *s: torch.randn(*s, dtype=torch.bfloat16) * 0.05)(B, S, D)),
            "w_gate": _w((lambda *s: torch.randn(*s, dtype=torch.bfloat16) * 0.05)(D, F)),
            "w_up": _w((lambda *s: torch.randn(*s, dtype=torch.bfloat16) * 0.05)(D, F)),
            "w_down": _w((lambda *s: torch.randn(*s, dtype=torch.bfloat16) * 0.05)(F, D)),
        }
    _B_in = _B_setup()

    return {
        "A_x": _A_in["x"],
        "A_Lambda_re": _A_in["Lambda_re"],
        "A_Lambda_im": _A_in["Lambda_im"],
        "A_B_re": _A_in["B_re"],
        "A_B_im": _A_in["B_im"],
        "A_C_re": _A_in["C_re"],
        "A_C_im": _A_in["C_im"],
        "A_log_dt": _A_in["log_dt"],
        "A_Dskip": _A_in["Dskip"],
        "B_x": _B_in["x"],
        "B_w_gate": _B_in["w_gate"],
        "B_w_up": _B_in["w_up"],
        "B_w_down": _B_in["w_down"],
        "gate_w": _w((lambda *s: torch.randn(*s, dtype=torch.bfloat16) * 0.05)(640) * 16.0),
    }

def reference(A_x, A_Lambda_re, A_Lambda_im, A_B_re, A_B_im, A_C_re, A_C_im, A_log_dt, A_Dskip, B_x, B_w_gate, B_w_up, B_w_down, gate_w):
    def _A_ref(x, Lambda_re, Lambda_im, B_re, B_im, C_re, C_im, log_dt, Dskip):
        Bsz, L, H = x.shape
        Lam = Lambda_re + 1j * Lambda_im
        Bmat = B_re + 1j * B_im
        Cmat = C_re + 1j * C_im
        dt = torch.exp(log_dt)
        Lam_bar = torch.exp(dt * Lam)
        B_bar = ((Lam_bar - 1.0) / Lam)[:, None] * Bmat
        Bu = torch.einsum("bld,pd->blp", x + 0j, B_bar)
        h = torch.zeros_like(Bu[:, 0, :])
        hs = []
        for t in range(L):
            h = Lam_bar[None, :] * h + Bu[:, t, :]
            hs.append(h)
        hseq = torch.stack(hs, 1)
        y = 2.0 * torch.einsum("blp,dp->bld", hseq, Cmat).real + x * Dskip[None, None, :]
        return y

    def _B_ref(x, w_gate, w_up, w_down):
        g = _silu(x @ w_gate)
        u = x @ w_up
        return (g * u) @ w_down

    x = A_x
    g = _sigmoid((x * gate_w).sum(-1, keepdims=True))
    a = _A_ref(x, A_Lambda_re, A_Lambda_im, A_B_re, A_B_im, A_C_re, A_C_im, A_log_dt, A_Dskip)
    b = _B_ref(x, B_w_gate, B_w_up, B_w_down)
    return g * a + (1 - g) * b
```

```cpp
#include <hip/hip_runtime.h>
#include <hip/hip_bf16.h>
#include <stdint.h>

typedef unsigned short u16;
__device__ __forceinline__ float bf2f(u16 v) { return __uint_as_float(((unsigned)v) << 16); }
__device__ __forceinline__ u16 f2bf(float f) { __hip_bfloat16 h = __float2bfloat16(f); return *(u16*)&h; }
__device__ __forceinline__ float rbf(float f) { return bf2f(f2bf(f)); }

constexpr int L = 2048, D = 640, P = 640, F = 1728;

__global__ void k_params(const u16* lre, const u16* lim, const u16* logdt, float4* prm) {
  int p = blockIdx.x * blockDim.x + threadIdx.x; if (p >= P) return;
  float dt = rbf(expf(bf2f(logdt[p])));
  float a = bf2f(lre[p]), b = bf2f(lim[p]);
  float zr = dt * a, zi = dt * b;
  float e = expf(zr); float lr = e * cosf(zi), li = e * sinf(zi);
  float nr = lr - 1.f, ni = li; float den = a * a + b * b;
  float cr = (nr * a + ni * b) / den, ci = (ni * a - nr * b) / den;
  prm[p] = make_float4(lr, li, cr, ci);
}
__global__ void k_gate(const u16* x, const u16* gw, float* g) {
  int t = blockIdx.x; int lane = threadIdx.x; float s = 0.f;
  for (int d = lane; d < D; d += 64) s += rbf(bf2f(x[t * D + d]) * bf2f(gw[d]));
  for (int o = 32; o; o >>= 1) s += __shfl_xor(s, o);
  if (lane == 0) { float z = rbf(s); g[t] = rbf(1.f / (1.f + expf(-z))); }
}
__global__ void k_bu(const u16* x, const u16* bre, const u16* bim, const float4* prm, float2* bu) {
  int p = blockIdx.x * blockDim.x + threadIdx.x; int t = blockIdx.y; if (p >= P) return;
  float sr = 0.f, si = 0.f;
  for (int d = 0; d < D; ++d) { float xv = bf2f(x[t * D + d]); sr += xv * bf2f(bre[p * D + d]); si += xv * bf2f(bim[p * D + d]); }
  float4 q = prm[p];
  bu[t * P + p] = make_float2(q.z * sr - q.w * si, q.z * si + q.w * sr);
}
__global__ void k_scan(const float4* prm, float2* bu) {
  int p = blockIdx.x * blockDim.x + threadIdx.x; if (p >= P) return;
  float4 q = prm[p]; float hr = 0.f, hi = 0.f;
  for (int t = 0; t < L; ++t) { float2 b = bu[t * P + p]; float nr = q.x * hr - q.y * hi + b.x, ni = q.x * hi + q.y * hr + b.y; hr = nr; hi = ni; bu[t * P + p] = make_float2(hr, hi); }
}
__global__ void k_act(const u16* x, const u16* wg, const u16* wu, u16* act) {
  int f = blockIdx.x * blockDim.x + threadIdx.x; int t = blockIdx.y; if (f >= F) return;
  float sg = 0.f, su = 0.f;
  for (int d = 0; d < D; ++d) { float xv = bf2f(x[t * D + d]); sg += xv * bf2f(wg[d * F + f]); su += xv * bf2f(wu[d * F + f]); }
  float g = rbf(sg), u = rbf(su);
  float s = rbf(g / (1.f + expf(-g)));
  act[t * F + f] = f2bf(s * u);
}
__global__ void k_out(const u16* x, const float2* h, const u16* cre, const u16* cim, const u16* dskip, const u16* act, const u16* wd, const float* g, float* out) {
  int d = blockIdx.x * blockDim.x + threadIdx.x; int t = blockIdx.y; if (d >= D) return;
  float s = 0.f;
  for (int p = 0; p < P; ++p) { float2 hv = h[t * P + p]; s += hv.x * bf2f(cre[d * P + p]) - hv.y * bf2f(cim[d * P + p]); }
  float a = 2.f * s + rbf(bf2f(x[t * D + d]) * bf2f(dskip[d]));
  float b = 0.f;
  for (int f = 0; f < F; ++f) b += bf2f(act[t * F + f]) * bf2f(wd[f * D + d]);
  b = rbf(b);
  float gv = g[t];
  out[t * D + d] = gv * a + rbf(rbf(1.f - gv) * b);
}

extern "C" void kernel_launch(void* const* d_in, const int* in_sizes, int n_in,
                              void* d_out, int out_size, void* d_ws, size_t ws_size,
                              hipStream_t stream) {
  const u16* x = (const u16*)d_in[0];
  const u16 *lre = (const u16*)d_in[1], *lim = (const u16*)d_in[2], *bre = (const u16*)d_in[3], *bim = (const u16*)d_in[4];
  const u16 *cre = (const u16*)d_in[5], *cim = (const u16*)d_in[6], *logdt = (const u16*)d_in[7], *dskip = (const u16*)d_in[8];
  const u16 *wg = (const u16*)d_in[10], *wu = (const u16*)d_in[11], *wd = (const u16*)d_in[12], *gw = (const u16*)d_in[13];
  char* ws = (char*)d_ws;
  float4* prm = (float4*)ws; float* g = (float*)(ws + 16384); float2* bu = (float2*)(ws + 65536); u16* act = (u16*)(ws + 65536 + (size_t)L * P * 8);
  k_params<<<(P + 63) / 64, 64, 0, stream>>>(lre, lim, logdt, prm);
  k_gate<<<L, 64, 0, stream>>>(x, gw, g);
  k_bu<<<dim3(P / 128, L), 128, 0, stream>>>(x, bre, bim, prm, bu);
  k_scan<<<P / 64, 64, 0, stream>>>(prm, bu);
  k_act<<<dim3((F + 127) / 128, L), 128, 0, stream>>>(x, wg, wu, act);
  k_out<<<dim3(D / 128, L), 128, 0, stream>>>(x, bu, cre, cim, dskip, act, wd, g, (float*)d_out);
}
```

```cpp
#include <hip/hip_runtime.h>
#include <stdint.h>
#include <utility>

typedef unsigned short u16;
typedef float f32x4 __attribute__((ext_vector_type(4)));
typedef float f32x2 __attribute__((ext_vector_type(2)));
typedef unsigned u32x4 __attribute__((ext_vector_type(4)));
typedef unsigned u32x2 __attribute__((ext_vector_type(2)));
typedef short s16x4 __attribute__((ext_vector_type(4)));
typedef short s16x8 __attribute__((ext_vector_type(8)));
typedef __bf16 bf16x2 __attribute__((ext_vector_type(2)));
#define DI __device__ __forceinline__
#define LAS __attribute__((address_space(3)))

constexpr int L_ = 2048, D_ = 640, P_ = 640, F_ = 1728;
constexpr int KSTEPS = 94;
constexpr unsigned AF_BYTES = 16u * KSTEPS * 8192u;
constexpr unsigned WF_BYTES = 8u * KSTEPS * 5120u;
constexpr size_t WS_AF = 0, WS_WF = AF_BYTES, WS_GATE = WS_WF + WF_BYTES, WS_XCH = WS_GATE + 8192;
constexpr unsigned XCH_BYTES = 4096u + 5u * 16u * 2048u;
constexpr unsigned HO_WORDS = 128u * 256u, HO_TILES = 128u * 40960u;
constexpr size_t WS_HO = WS_XCH + XCH_BYTES;
constexpr int WT = 16;

DI float bf_lo(unsigned v) { return __uint_as_float(v << 16); }
DI float bf_hi(unsigned v) { return __uint_as_float(v & 0xffff0000u); }
DI float bf2f(u16 v) { return __uint_as_float(((unsigned)v) << 16); }
DI unsigned cvtpk(float lo, float hi) { f32x2 v = {lo, hi}; bf16x2 b = __builtin_convertvector(v, bf16x2); return __builtin_bit_cast(unsigned, b); }
DI float rbf(float f) { return bf_lo(cvtpk(f, f)); }
DI u32x4 make_srd(const void* p, unsigned bytes) {
  const unsigned long long a = (unsigned long long)p;
  u32x4 r; r[0] = (unsigned)a; r[1] = (unsigned)(a >> 32) & 0xffffu; r[2] = bytes; r[3] = 0x00020000u; return r;
}

#define store16(data, rsrc, voff, aux) __builtin_amdgcn_raw_buffer_store_b128((data), (rsrc), (voff), 0, (aux))

struct K1Args;
namespace k1 {
enum { V_T1 = 0, V_T5 = 1, V_SSM = 2 };
constexpr int LDS_BYTES = 163840;
constexpr int NSTAGE = 10;
constexpr unsigned XROW = 1280, WROW = 3456;
constexpr int SS_E = 147456, SS_PRM = SS_E + 8192, SS_POW = SS_PRM + 1024, SS_X = 0;

template <int V> struct Cfg;
template <> struct Cfg<V_T1>  { static constexpr int NX = 5, XS = 3, WS = 3, XB = 20480, WB = 32768, XSTR = 53248, WSTR = 53248, WOFF = 20480, NLX = 3, NLW = 4; };
template <> struct Cfg<V_T5>  { static constexpr int NX = 5, XS = 2, WS = 3, XB = 40960, WB = 16384, XSTR = 40960, WSTR = 16384, WOFF = 81920, NLX = 5, NLW = 2; };
template <> struct Cfg<V_SSM> { static constexpr int NX = 4, XS = 3, WS = 3, XB = 16384, WB = 32768, XSTR = 49152, WSTR = 49152, WOFF = 16384, NLX = 2, NLW = 4; };

struct Side {
  u32x4 xa[2], ga; unsigned xb[2], gb, pr[3];
  u32x4 srdX, srdG, srdA, srdB, srdP[3];
  unsigned v16, v4, vw, vp, sx[2], sw[4], sp, m0;
};
template <int I> DI void side_ld(Side& s);
constexpr int NSIDE = 13;
struct Ctx {
  u32x4 srdX, srdW0, srdW1;
  unsigned vx, vx3, vw;
  unsigned sx[5];
  unsigned sx3, sw;
  unsigned w;
  unsigned rx, rw0, rw1;
  LAS char* lds;
  f32x4 acc[5][4];
  Side* side;
};

DI void glds(unsigned m0, unsigned voff, u32x4 srd, unsigned soff) {
  asm volatile("s_mov_b32 m0, %0\n\ts_nop 0\n\tbuffer_load_dwordx4 %1, %2, %3 offen lds" :: "s"(m0), "v"(voff), "s"(srd), "s"(soff) : "memory");
}
DI void glds_half(unsigned m0, unsigned voff, u32x4 srd, unsigned soff) {
  asm volatile("s_mov_b32 m0, %0\n\ts_mov_b32 exec_hi, 0\n\tbuffer_load_dwordx4 %1, %2, %3 offen lds\n\ts_mov_b32 exec_hi, -1" :: "s"(m0), "v"(voff), "s"(srd), "s"(soff) : "memory");
}
template <int N> DI void waitv() { asm volatile("s_waitcnt vmcnt(%c0)" :: "i"(N) : "memory"); }

template <int V, int S, int I> DI void ldx(Ctx& c) {
  using C = Cfg<V>;
  constexpr unsigned slot = (S % C::XS) * C::XSTR;
  if constexpr (V == V_T1 && I == 2) glds_half(slot + 16384u + c.w * 512u, c.vx3, c.srdX, c.sx3 + S * 128u);
  else glds(slot + (c.w + 8u * I) * 1024u, c.vx, c.srdX, c.sx[I] + S * 128u);
}
template <int V, int S, int I> DI void ldw(Ctx& c) {
  using C = Cfg<V>;
  constexpr unsigned slot = C::WOFF + (S % C::WS) * C::WSTR;
  if constexpr (V == V_T1) {
    glds(slot + (I >> 1) * 16384u + (2u * c.w + (I & 1)) * 1024u, c.vw, (I >> 1) ? c.srdW1 : c.srdW0, c.sw + (S * 64u + (I & 1) * 4u) * WROW);
  } else if constexpr (V == V_T5) {
    glds(slot + I * 8192u + c.w * 1024u, c.vw, I ? c.srdW1 : c.srdW0, c.sw + S * 64u * WROW);
  } else {
    glds(slot + (I >> 1) * 16384u + (c.w + 8u * (I & 1)) * 1024u, c.vw, (I >> 1) ? c.srdW1 : c.srdW0, c.sw + (I & 1) * 64u * XROW + S * 128u);
  }
}
struct Frags { s16x8 x[5]; s16x8 w[4]; s16x4 wl[4], wh[4]; };

template <int V, int S, int KK, int I> DI void rd(Ctx& c, Frags& f) {
  using C = Cfg<V>;
  if constexpr (I < C::NX) {
    constexpr unsigned off = (S % C::XS) * C::XSTR + I * 2048u;
    f.x[I] = *(LAS const s16x8*)(c.lds + ((c.rx ^ (KK * 64u)) + off));
  } else if constexpr (V == V_SSM) {
    constexpr int n = I - C::NX;
    constexpr unsigned off = C::WOFF + (S % C::WS) * C::WSTR + (n & 1) * 16384u + (n >> 1) * 2048u;
    f.w[n] = *(LAS const s16x8*)(c.lds + ((c.rw0 ^ (KK * 64u)) + off));
  } else {
    constexpr int j = I - C::NX, n = j >> 1, half = j & 1;
    constexpr unsigned rowb = (V == V_T1) ? 256u : 128u, matb = (V == V_T1) ? 16384u : 8192u;
    constexpr unsigned off = C::WOFF + (S % C::WS) * C::WSTR + (n & 1) * matb + (KK * 32u + half * 4u) * rowb;
    const s16x4 v = __builtin_amdgcn_ds_read_tr16_b64_v4i16((LAS s16x4*)(c.lds + (((n >> 1) ? c.rw1 : c.rw0) + off)));
    if constexpr (half == 0) f.wl[n] = v; else { f.wh[n] = v; f.w[n] = __builtin_shufflevector(f.wl[n], v, 0, 1, 2, 3, 4, 5, 6, 7); }
  }
}
template <int V> constexpr int nreads() { return V == V_SSM ? 8 : 13; }
template <int V> constexpr int nmm() { return Cfg<V>::NX * 4; }
template <int V, bool FIRST, int I> DI void mm(Ctx& c, Frags& f) {
  constexpr int m = I % Cfg<V>::NX, n = I / Cfg<V>::NX;
  if constexpr (V == V_SSM) {
    if constexpr (FIRST) asm volatile("v_mfma_f32_16x16x32_bf16 %0, %1, %2, 0" : "=v"(c.acc[m][n]) : "v"(f.x[m]), "v"(f.w[n]) : "memory");
    else asm volatile("v_mfma_f32_16x16x32_bf16 %0, %1, %2, %0" : "+v"(c.acc[m][n]) : "v"(f.x[m]), "v"(f.w[n]) : "memory");
  } else {
    if constexpr (FIRST) asm volatile("v_mfma_f32_16x16x32_bf16 %0, %1, %2, 0" : "=v"(c.acc[m][n]) : "v"(f.w[n]), "v"(f.x[m]) : "memory");
    else asm volatile("v_mfma_f32_16x16x32_bf16 %0, %1, %2, %0" : "+v"(c.acc[m][n]) : "v"(f.w[n]), "v"(f.x[m]) : "memory");
  }
}
template <int V, int S, int I> DI void half0_item(Ctx& c, Frags& fa, Frags& fb) {
  using C = Cfg<V>;
  mm<V, S == 0, I>(c, fa);
  if constexpr (I < nreads<V>()) rd<V, S, 1, I>(c, fb);
  if constexpr (S == 0) {
    constexpr int NX2 = (C::XS == 3) ? C::NLX : 0, J = I - (nmm<V>() - NX2 - C::NLW);
    if constexpr (J >= 0 && J < NX2) ldx<V, 2, J>(c);
    else if constexpr (J >= NX2) ldw<V, 2, J - NX2>(c);
  }
}
template <int V, int S, int... I> DI void half0(Ctx& c, Frags& fa, Frags& fb, std::integer_sequence<int, I...>) { (half0_item<V, S, I>(c, fa, fb), ...); }
template <int V, int S, int J> DI void stage_ld(Ctx& c) {
  using C = Cfg<V>;
  if constexpr (J < C::NLX) { if constexpr (S + C::XS < NSTAGE) ldx<V, S + C::XS, J>(c); }
  else if constexpr (J < C::NLX + C::NLW) { if constexpr (S + C::WS < NSTAGE) ldw<V, S + C::WS, J - C::NLX>(c); }
}
template <int V, int S, int I> DI void half1_item(Ctx& c, Frags& fa, Frags& fb) {
  mm<V, false, I>(c, fb);
  if constexpr (S + 1 < NSTAGE && I < nreads<V>()) rd<V, S + 1, 0, I>(c, fa);
  constexpr int J = I - (nmm<V>() - (Cfg<V>::NLX + Cfg<V>::NLW));
  if constexpr (J >= 0) stage_ld<V, S, J>(c);
  if constexpr (V != V_SSM && S == 8 && I % 3 == 2 && I / 3 < 6) side_ld<I / 3>(*c.side);
}
template <int V, int S, int... I> DI void half1(Ctx& c, Frags& fa, Frags& fb, std::integer_sequence<int, I...>) { (half1_item<V, S, I>(c, fa, fb), ...); }

template <int V, int S> DI void stage(Ctx& c, Frags& fa, Frags& fb) {
  using C = Cfg<V>;
  half0<V, S>(c, fa, fb, std::make_integer_sequence<int, nmm<V>()>{});
  if constexpr (S + 1 < NSTAGE) {
    constexpr int nx2 = (S + 2 < NSTAGE) ? C::NLX : 0, nw2 = (S + 2 < NSTAGE) ? C::NLW : 0;
    constexpr int n = (V == V_T5) ? nw2 : nx2 + nw2;
    asm volatile("s_waitcnt vmcnt(%c0) lgkmcnt(0)\n\ts_barrier" :: "i"(n) : "memory");
  } else {
    asm volatile("s_waitcnt lgkmcnt(0)" ::: "memory");
  }
  half1<V, S>(c, fa, fb, std::make_integer_sequence<int, nmm<V>()>{});
}
template <int V, int... S> DI void stages(Ctx& c, Frags& fa, Frags& fb, std::integer_sequence<int, S...>) { (stage<V, S>(c, fa, fb), ...); }

template <int V> struct LastOrder;
template <> struct LastOrder<V_T1>  { static constexpr int kk[10] = {0, 0, 0, 1, 1, 0, 1, 0, 1, 1}, m[10] = {0, 1, 2, 0, 1, 3, 2, 4, 3, 4}; };
template <> struct LastOrder<V_T5>  { static constexpr int kk[10] = {0, 0, 0, 1, 1, 0, 1, 0, 1, 1}, m[10] = {0, 1, 2, 0, 1, 3, 2, 4, 3, 4}; };
template <> struct LastOrder<V_SSM> { static constexpr int kk[8] = {0, 0, 1, 0, 1, 0, 1, 1}, m[8] = {0, 1, 0, 2, 1, 3, 2, 3}; };
template <int V, int I> DI void last_mm(Ctx& c, Frags& fa, Frags& fb) {
  constexpr int kk = LastOrder<V>::kk[I >> 2], m = LastOrder<V>::m[I >> 2], n = I & 3;
  mm<V, false, n * Cfg<V>::NX + m>(c, kk ? fb : fa);
  if constexpr (I < nreads<V>()) rd<V, NSTAGE - 1, 1, I>(c, fb);
  if constexpr (V != V_SSM && I % 3 == 1 && I / 3 < 7) side_ld<6 + I / 3>(*c.side);
}
template <int V, int S, int... I> DI void ldx_all(Ctx& c, std::integer_sequence<int, I...>) { (ldx<V, S, I>(c), ...); }
template <int V, int S, int... I> DI void ldw_all(Ctx& c, std::integer_sequence<int, I...>) { (ldw<V, S, I>(c), ...); }
template <int V, int... I> DI void rd_first(Ctx& c, Frags& f, std::integer_sequence<int, I...>) { (rd<V, 0, 0, I>(c, f), ...); }

template <int V> DI void prologue_loads(Ctx& c) {
  using C = Cfg<V>;
  ldx_all<V, 0>(c, std::make_integer_sequence<int, C::NLX>{}); ldw_all<V, 0>(c, std::make_integer_sequence<int, C::NLW>{});
  ldx_all<V, 1>(c, std::make_integer_sequence<int, C::NLX>{}); ldw_all<V, 1>(c, std::make_integer_sequence<int, C::NLW>{});
}
template <int V> constexpr int n_prologue() { return 2 * (Cfg<V>::NLX + Cfg<V>::NLW); }
template <int V> DI void mainloop(Ctx& c, Frags& fa, Frags& fb) {
  asm volatile("s_waitcnt vmcnt(%c0)\n\ts_barrier" :: "i"(Cfg<V>::NLX + Cfg<V>::NLW) : "memory");
  rd_first<V>(c, fa, std::make_integer_sequence<int, nreads<V>()>{});
  stages<V>(c, fa, fb, std::make_integer_sequence<int, NSTAGE - 1>{});
}

DI unsigned mt_token0(unsigned mt) { return 64u * (mt >> 2) + 32u * ((mt >> 1) & 1) + 8u * (mt & 1); }

DI f32x2 cfma(f32x2 a, f32x2 b, f32x2 c) {
  float r, i;
  asm("v_fma_f32 %0, %2, %4, %6\n\tv_fma_f32 %1, %2, %5, %7\n\tv_fma_f32 %0, -%3, %5, %0\n\tv_fma_f32 %1, %3, %4, %1"
      : "=&v"(r), "=&v"(i) : "v"(a[0]), "v"(a[1]), "v"(b[0]), "v"(b[1]), "v"(c[0]), "v"(c[1]));
  return (f32x2){r, i};
}
DI f32x2 cmul(f32x2 a, f32x2 b) {
  float r, i;
  asm("v_mul_f32 %0, %2, %4\n\tv_mul_f32 %1, %2, %5\n\tv_fma_f32 %0, -%3, %5, %0\n\tv_fma_f32 %1, %3, %4, %1"
      : "=&v"(r), "=&v"(i) : "v"(a[0]), "v"(a[1]), "v"(b[0]), "v"(b[1]));
  return (f32x2){r, i};
}
DI f32x2 cadd(f32x2 a, f32x2 b) {
  float r, i;
  asm("v_add_f32 %0, %2, %4\n\tv_add_f32 %1, %3, %5" : "=&v"(r), "=&v"(i) : "v"(a[0]), "v"(a[1]), "v"(b[0]), "v"(b[1]));
  return (f32x2){r, i};
}
struct Params { float lr, li, cr, ci; };
DI void sincos_f(const float x, float& sn, float& cs) {
  if (__builtin_expect(__any(fabsf(x) > 16384.f), 0)) { sincosf(x, &sn, &cs); return; }
  const float k = rintf(x * 0.6366197723675814f);
  float r = fmaf(k, -1.5707963705062866f, x); r = fmaf(k, 4.371138828673793e-08f, r); r = fmaf(k, 1.7151245100058819e-15f, r);
  const float z = r * r;
  const float s = fmaf(r * z, fmaf(z, fmaf(z, fmaf(z, 2.7557314297e-06f, -1.9841270114e-04f), 8.3333337680e-03f), -1.6666667163e-01f), r);
  const float c = fmaf(z * z, fmaf(z, fmaf(z, fmaf(z, -2.7557314297e-07f, 2.4801587642e-05f), -1.3888889225e-03f), 4.1666667908e-02f), fmaf(z, -0.5f, 1.f));
  const int n = (int)k;
  const float a = (n & 1) ? c : s, b = (n & 1) ? s : c;
  sn = (n & 2) ? -a : a; cs = ((n + 1) & 2) ? -b : b;
}
DI Params ssm_params(float lre, float lim, float logdt) {
  const float dt = rbf(expf(logdt));
  const float zr = dt * lre, zi = dt * lim;
  const float e = expf(zr); float s, co; sincos_f(zi, s, co);
  Params p; p.lr = e * co; p.li = e * s;
  const f32x2 num = cmul((f32x2){p.lr - 1.f, p.li}, (f32x2){lre, -lim}), dd = cmul((f32x2){lre, lim}, (f32x2){lre, -lim});
  p.cr = num[0] / dd[0]; p.ci = num[1] / dd[0];
  return p;
}
}

struct K1Args {
  const u16 *x, *lre, *lim, *bre, *bim, *cre, *cim, *logdt, *wg, *wu, *wd, *gw;
  char* ws;
};
struct K1Map { unsigned v[256]; };


extern "C" __global__ void zz_code_end();
DI void code_touch(const unsigned slot, const unsigned w, const int lane) {
  unsigned long long pc, end;
  asm volatile("s_getpc_b64 %0" : "=s"(pc));
  asm volatile("s_getpc_b64 s[98:99]\n\ts_add_u32 s98, s98, zz_code_end@rel32@lo+4\n\ts_addc_u32 s99, s99, zz_code_end@rel32@hi+12\n\ts_mov_b64 %0, s[98:99]" : "=s"(end) :: "scc", "s98", "s99");
  const unsigned long long base = pc & ~127ull;
  unsigned span = (unsigned)end - (unsigned)base; span = span < (1u << 17) ? span : 0;
  span = __builtin_amdgcn_readfirstlane(span);
  const u32x4 srd = make_srd((const void*)base, span);
  const unsigned voff = lane < 2 ? ((slot * 8 + w) * 2 + lane) * 128u : 0x40000000u;
  asm volatile("s_mov_b32 m0, %0\n\ts_nop 4\n\tbuffer_load_dword %1, %2, 0 offen lds" :: "s"(163840u - 256u), "v"(voff), "s"(srd) : "memory");
}

DI void side_setup(const K1Args& a, k1::Side& s, const unsigned j, const int lane, const unsigned ldsbase) {
  const bool isC = j < 800;
  const unsigned dl = lane & 15;
  s.m0 = ldsbase;
  s.srdX = make_srd(a.x, L_ * D_ * 2); s.srdG = make_srd(a.gw, D_ * 2);
  s.srdA = make_srd(isC ? a.cre : a.wd, isC ? D_ * P_ * 2 : F_ * D_ * 2); s.srdB = make_srd(isC ? a.cim : a.wd, isC ? D_ * P_ * 2 : F_ * D_ * 2);
  s.srdP[0] = make_srd(a.lre, P_ * 2); s.srdP[1] = make_srd(a.lim, P_ * 2); s.srdP[2] = make_srd(a.logdt, P_ * 2);
  s.v16 = lane * 16; s.v4 = lane * 4; s.vp = dl * 2;
  s.vw = isC ? (lane >> 1) * 1280u + (lane & 1) * 16u : (lane >> 2) * 1280u + (lane & 3) * 16u;
  s.sx[0] = j * 1280u; s.sx[1] = (j + 1408u) * 1280u;
  const unsigned cn = j / 20, np = j % 20, soC = np * 32u * 1280u + cn * 32u;
  const unsigned e0 = 2 * (j - 800), e1 = e0 + 1;
  const unsigned so0 = e0 < 1080 ? (e0 / 20) * 32u * 1280u + (e0 % 20) * 64u : 0x40000000u, so1 = e1 < 1080 ? (e1 / 20) * 32u * 1280u + (e1 % 20) * 64u : 0x40000000u;
  s.sw[0] = isC ? soC : so0;
  s.sw[1] = isC ? soC : so0 + 16u * 1280u;
  s.sw[2] = isC ? 0x40000000u : so1;
  s.sw[3] = isC ? 0x40000000u : so1 + 16u * 1280u;
  s.sp = isC ? cn * 32u : 0x40000000u;
}
namespace k1 {
template <int I> DI void side_ld(Side& s) {
  if constexpr (I < 4) glds(s.m0 + I * 1024u, s.vw, (I & 1) ? s.srdB : s.srdA, s.sw[I]);
  if constexpr (I >= 4 && I < 7) asm volatile("buffer_load_ushort %0, %1, %2, %3 offen" : "=v"(s.pr[I - 4]) : "v"(s.vp), "s"(s.srdP[I - 4]), "s"(s.sp) : "memory");
  if constexpr (I == 7) asm volatile("buffer_load_dwordx4 %0, %1, %2, 0 offen" : "=v"(s.ga) : "v"(s.v16), "s"(s.srdG) : "memory");
  if constexpr (I == 8) asm volatile("buffer_load_dword %0, %1, %2, 0 offen offset:1024" : "=v"(s.gb) : "v"(s.v4), "s"(s.srdG) : "memory");
  if constexpr (I == 9 || I == 11) asm volatile("buffer_load_dwordx4 %0, %1, %2, %3 offen" : "=v"(s.xa[(I - 9) / 2]) : "v"(s.v16), "s"(s.srdX), "s"(s.sx[(I - 9) / 2]) : "memory");
  if constexpr (I == 10 || I == 12) asm volatile("buffer_load_dword %0, %1, %2, %3 offen offset:1024" : "=v"(s.xb[(I - 10) / 2]) : "v"(s.v4), "s"(s.srdX), "s"(s.sx[(I - 10) / 2]) : "memory");
}
}
DI float dot2bf(const unsigned a, const unsigned b, const float c) { return __builtin_amdgcn_fdot2_f32_bf16(__builtin_bit_cast(bf16x2, a), __builtin_bit_cast(bf16x2, b), c, false); }
template <int CTRL, int ROWMASK> DI float dpp_add(float v) {
  return v + __uint_as_float(__builtin_amdgcn_update_dpp(0u, __float_as_uint(v), CTRL, ROWMASK, 0xf, true));
}
DI float wave_sum63(float v) {
  v = dpp_add<0x111, 0xf>(v); v = dpp_add<0x112, 0xf>(v); v = dpp_add<0x114, 0xf>(v); v = dpp_add<0x118, 0xf>(v);
  v = dpp_add<0x142, 0xa>(v); v = dpp_add<0x143, 0xc>(v);
  return v;
}
typedef __bf16 bf16x8 __attribute__((ext_vector_type(8)));
DI f32x4 mfma_sel(u32x4 a, s16x8 sel) { const f32x4 z = {0.f, 0.f, 0.f, 0.f}; return __builtin_amdgcn_mfma_f32_16x16x32_bf16(__builtin_bit_cast(bf16x8, a), __builtin_bit_cast(bf16x8, sel), z, 0, 0, 0); }
DI void make_sel(s16x8& S0, s16x8& S1, const int lane) {
  const unsigned tl = lane & 15, q = lane >> 4;
  const bool on0 = (q >> 1) == 0 && (tl >> 3) == (q & 1), on1 = (q >> 1) == 1 && (tl >> 3) == (q & 1);
#pragma unroll
  for (int j = 0; j < 8; ++j) { const bool e = (tl & 7) == (unsigned)j; S0[j] = (on0 && e) ? (short)0x3f80 : (short)0; S1[j] = (on1 && e) ? (short)0x3f80 : (short)0; }
}
DI void side_finish(const K1Args& a, k1::Side& s, LAS char* lds, const unsigned j, const int lane) {
  const __amdgpu_buffer_rsrc_t rW = __builtin_amdgcn_make_buffer_rsrc(a.ws + WS_WF, 0, WF_BYTES, 0x00020000);
  const __amdgpu_buffer_rsrc_t rG = __builtin_amdgcn_make_buffer_rsrc(a.ws + WS_GATE, 0, L_ * 4, 0x00020000);
  const unsigned q = lane >> 4;
  if (j < 800) {
    const unsigned cn = j / 20, np = j % 20;
    const k1::Params p = k1::ssm_params(bf_lo(s.pr[0]), bf_lo(s.pr[1]), bf_lo(s.pr[2]));
    float cr[4], ci[4];
#pragma unroll
    for (int r = 0; r < 4; ++r) { cr[r] = __shfl(2.f * p.cr, 4 * q + r); ci[r] = __shfl(2.f * p.ci, 4 * q + r); }
#pragma unroll
    for (int i = 0; i < 2; ++i) {
      const u32x2 re = *(LAS const u32x2*)(lds + s.m0 + (16 * i + (lane & 15)) * 32 + q * 8), im = *(LAS const u32x2*)(lds + s.m0 + 1024 + (16 * i + (lane & 15)) * 32 + q * 8);
      const float vr[4] = {bf_lo(re[0]), bf_hi(re[0]), bf_lo(re[1]), bf_hi(re[1])}, vi[4] = {bf_lo(im[0]), bf_hi(im[0]), bf_lo(im[1]), bf_hi(im[1])};
      float o0[4], o1[4];
#pragma unroll
      for (int r = 0; r < 4; ++r) { const f32x2 z = k1::cmul((f32x2){cr[r], ci[r]}, (f32x2){vr[r], vi[r]}); o0[r] = z[0]; o1[r] = -z[1]; }
      const u32x4 o = {cvtpk(o0[0], o0[1]), cvtpk(o0[2], o0[3]), cvtpk(o1[0], o1[1]), cvtpk(o1[2], o1[3])};
      const unsigned nt = 2 * np + i, dg = nt / 5, n = nt % 5;
      store16(o, rW, lane * 16 + ((dg * KSTEPS + cn) * 5 + n) * 1024u, WT);
    }
  } else {
    s16x8 S0, S1; make_sel(S0, S1, lane);
#pragma unroll
    for (int r = 0; r < 2; ++r) {
      const unsigned e = 2 * (j - 800) + r, ks = e / 20, np = e % 20;
      const u32x4 wa = *(LAS const u32x4*)(lds + s.m0 + 2048 * r + (lane & 15) * 64 + q * 16), wb = *(LAS const u32x4*)(lds + s.m0 + 2048 * r + 1024 + (lane & 15) * 64 + q * 16);
#pragma unroll
      for (int i = 0; i < 2; ++i) {
        const f32x4 da = mfma_sel(wa, i ? S1 : S0), db = mfma_sel(wb, i ? S1 : S0);
        const u32x4 o = {cvtpk(da[0], da[1]), cvtpk(da[2], da[3]), cvtpk(db[0], db[1]), cvtpk(db[2], db[3])};
        const unsigned nt = 2 * np + i, dg = nt / 5, n = nt % 5;
        store16(o, rW, e < 1080 ? lane * 16 + ((dg * KSTEPS + 40 + ks) * 5 + n) * 1024u : 0x40000000u, WT);
      }
    }
  }
  float sum[2];
#pragma unroll
  for (int r = 0; r < 2; ++r) {
    sum[r] = dot2bf(s.xb[r], s.gb, 0.f);
#pragma unroll
    for (int i = 0; i < 4; ++i) sum[r] = dot2bf(s.xa[r][i], s.ga[i], sum[r]);
  }
#pragma unroll
  for (int r = 0; r < 2; ++r) sum[r] = wave_sum63(sum[r]);
  const float z0 = __uint_as_float(__builtin_amdgcn_readlane(__float_as_uint(sum[0]), 63)), z1 = __uint_as_float(__builtin_amdgcn_readlane(__float_as_uint(sum[1]), 63));
  const float z = lane == 0 ? z0 : z1;
  const float g = rbf(1.f / (1.f + expf(-rbf(z))));
  __builtin_amdgcn_raw_buffer_store_b32(__float_as_uint(g), rG, lane < 2 ? (j + 1408u * lane) * 4u : 0x40000000u, 0, WT);
}

namespace k1 {
struct Epi { __amdgpu_buffer_rsrc_t rA; unsigned so[5], v16; u32x4 o[5]; };
template <int M, int P> DI void epi_piece(Ctx& c, Epi& e) {
  constexpr int ft = P >> 1, r = 2 * (P & 1);
  if constexpr ((P & 1) == 0) asm volatile("" : "+v"(c.acc[M][2 * ft]), "+v"(c.acc[M][2 * ft + 1]));
  unsigned pk; float t0, t1, p0, p1;
  asm volatile("v_mul_f32 %1, 0xbfb8aa3b, %5\n\tv_mul_f32 %2, 0xbfb8aa3b, %6\n\tv_exp_f32 %1, %1\n\tv_exp_f32 %2, %2\n\t"
               "v_mul_f32 %3, %5, %7\n\tv_mul_f32 %4, %6, %8\n\tv_add_f32 %1, 1.0, %1\n\tv_add_f32 %2, 1.0, %2\n\t"
               "v_rcp_f32 %1, %1\n\tv_rcp_f32 %2, %2\n\tv_mul_f32 %3, %3, %1\n\tv_mul_f32 %4, %4, %2\n\tv_cvt_pk_bf16_f32 %0, %3, %4"
               : "=&v"(pk), "=&v"(t0), "=&v"(t1), "=&v"(p0), "=&v"(p1)
               : "v"(c.acc[M][2 * ft][r]), "v"(c.acc[M][2 * ft][r + 1]), "v"(c.acc[M][2 * ft + 1][r]), "v"(c.acc[M][2 * ft + 1][r + 1]));
  e.o[M][P] = pk;
  if constexpr (P == 3) store16(e.o[M], e.rA, e.v16 + e.so[M], WT);
}
template <int V, int I> DI void ffn_last_item(Ctx& c, Frags& fa, Frags& fb, Epi& e) {
  last_mm<V, I>(c, fa, fb);
  if constexpr (I >= 21 && I <= 24) epi_piece<0, I - 21>(c, e);
  if constexpr (I >= 25 && I <= 28) epi_piece<1, I - 25>(c, e);
  if constexpr (I >= 33 && I <= 36) epi_piece<2, I - 33>(c, e);
}
template <int V, int... I> DI void ffn_last(Ctx& c, Frags& fa, Frags& fb, Epi& e, std::integer_sequence<int, I...>) { (ffn_last_item<V, I>(c, fa, fb, e), ...); }
}
template <int V> DI void ffn_body(const K1Args& a, LAS char* lds, const unsigned w, const int lane, const unsigned tile, const unsigned grp, const unsigned sjob) {
  using namespace k1;
  using C = Cfg<V>;
  Ctx c; c.lds = lds; c.w = w;
  const unsigned wm = (V == V_T1) ? (w >> 2) : (w >> 1), wn = (V == V_T1) ? (w & 3) : (w & 1);
  const unsigned mt0 = tile * ((V == V_T1) ? 10u : 20u);
  const unsigned f0 = (V == V_T1) ? grp * 128u : 1664u;
  c.srdX = make_srd(a.x, L_ * D_ * 2); c.srdW0 = make_srd(a.wg, D_ * F_ * 2); c.srdW1 = make_srd(a.wu, D_ * F_ * 2);
  const unsigned th = w & 1, r8 = lane >> 3, c8 = lane & 7;
  c.vx = r8 * XROW + ((c8 ^ (4 * th + (r8 >> 1))) * 16);
#pragma unroll
  for (int i = 0; i < (V == V_T1 ? 2 : 5); ++i) { unsigned mt = mt0 + (w >> 1) + 4 * i; mt = mt < 128 ? mt : 127; c.sx[i] = (mt_token0(mt) + 16 * th) * XROW; }
  if constexpr (V == V_T1) {
    const unsigned p = 16 + (w >> 1), th3 = p & 1, r = 4 * (w & 1) + (r8 & 3);
    unsigned mt = mt0 + (p >> 1); mt = mt < 128 ? mt : 127;
    c.vx3 = r * XROW + ((c8 ^ (4 * th3 + (r >> 1))) * 16);
    c.sx3 = (mt_token0(mt) + 16 * th3) * XROW;
    const unsigned kr = lane >> 4, pos = lane & 15, sz = kr | ((w & 1) << 2);
    c.vw = kr * WROW + ((pos ^ (sz << 1)) * 16);
    c.sw = 8 * w * WROW + f0 * 2;
  } else {
    const unsigned sz = ((r8 >> 1) & 1) | ((w & 1) << 1);
    c.vw = r8 * WROW + ((c8 ^ (sz << 1)) * 16);
    c.sw = 8 * w * WROW + f0 * 2;
  }
  const unsigned tl = lane & 15, q = lane >> 4;
  c.rx = (wm * 80 + tl) * 128 + ((q ^ (tl >> 1)) * 16);
  {
    const unsigned qq = tl >> 2, p = lane & 3;
    if constexpr (V == V_T1) {
      const unsigned sz = qq | ((q & 1) << 2);
      c.rw0 = (8 * q + qq) * 256 + (((4 * wn + (p >> 1)) ^ (sz << 1)) * 16) + (p & 1) * 8;
    } else {
      const unsigned sz = ((qq >> 1) & 1) | ((q & 1) << 1);
      c.rw0 = (8 * q + qq) * 128 + (((4 * wn + (p >> 1)) ^ (sz << 1)) * 16) + (p & 1) * 8;
    }
    c.rw1 = c.rw0 ^ 32;
  }
  Side s; c.side = &s;
  prologue_loads<V>(c);
  side_setup(a, s, sjob, lane, (V == V_T1 ? 53248u : 0u) + w * 4096u);
  Frags fa, fb;
  mainloop<V>(c, fa, fb);
  Epi e; e.rA = __builtin_amdgcn_make_buffer_rsrc(a.ws + WS_AF, 0, AF_BYTES, 0x00020000);
  {
    const unsigned ks = 40 + ((V == V_T1) ? grp * 4 : 52) + wn;
#pragma unroll
    for (int m = 0; m < 5; ++m) { const unsigned mt = mt0 + wm * 5 + m; e.so[m] = mt < 128 ? (((mt >> 3) * KSTEPS + ks) * 8 + (mt & 7)) * 1024u : 0x40000000u; }
    e.v16 = lane * 16;
  }
  ffn_last<V>(c, fa, fb, e, std::make_integer_sequence<int, 40>{});
  asm volatile("s_nop 3" ::: "memory");
  epi_piece<3, 0>(c, e); epi_piece<3, 1>(c, e); epi_piece<3, 2>(c, e); epi_piece<3, 3>(c, e);
  epi_piece<4, 0>(c, e); epi_piece<4, 1>(c, e); epi_piece<4, 2>(c, e); epi_piece<4, 3>(c, e);
  asm volatile("s_waitcnt vmcnt(5)" : "+v"(s.xa[0]), "+v"(s.xa[1]), "+v"(s.xb[0]), "+v"(s.xb[1]), "+v"(s.ga), "+v"(s.gb), "+v"(s.pr[0]), "+v"(s.pr[1]), "+v"(s.pr[2]) :: "memory");
  side_finish(a, s, lds, sjob, lane);
}

namespace k1 {
struct Scan { f32x2 lam[2], e[2]; };
template <int M, int R> DI void scan_piece(Ctx& c, Scan& s) {
  if constexpr (R == 0) asm volatile("" : "+v"(c.acc[M][0]), "+v"(c.acc[M][1]), "+v"(c.acc[M][2]), "+v"(c.acc[M][3]));
#pragma unroll
  for (int cn = 0; cn < 2; ++cn) {
    const f32x2 in = {c.acc[M][2 * cn][R], c.acc[M][2 * cn + 1][R]};
    if constexpr (M == 0 && R == 0) s.e[cn] = in; else s.e[cn] = cfma(s.lam[cn], s.e[cn], in);
  }
  asm volatile("" : "+v"(s.e[0]), "+v"(s.e[1]));
}
template <int I> DI void ssm_last_item(Ctx& c, Frags& fa, Frags& fb, Scan& s) {
  last_mm<V_SSM, I>(c, fa, fb);
  if constexpr (I >= 19 && I <= 22) scan_piece<0, I - 19>(c, s);
  if constexpr (I >= 27 && I <= 30) scan_piece<1, I - 27>(c, s);
}
template <int... I> DI void ssm_last(Ctx& c, Frags& fa, Frags& fb, Scan& s, std::integer_sequence<int, I...>) { (ssm_last_item<I>(c, fa, fb, s), ...); }
}
template <int DUMMY> DI void ssm_body(const K1Args& a, LAS char* lds, const unsigned w, const int lane, const unsigned tile, const unsigned grp) {
  using namespace k1;
  constexpr int V = V_SSM;
  Ctx c; c.lds = lds; c.w = w;
  const unsigned wm = w >> 2, wn = w & 3;
  c.srdX = make_srd(a.x, L_ * D_ * 2); c.srdW0 = make_srd(a.bre, P_ * D_ * 2); c.srdW1 = make_srd(a.bim, P_ * D_ * 2);
  const unsigned th = w & 1, r8 = lane >> 3, c8 = lane & 7;
  const unsigned swz8 = (c8 ^ (4 * th + (r8 >> 1))) * 16;
  c.vx = (16 * (r8 >> 2) + (r8 & 3)) * XROW + swz8;
#pragma unroll
  for (int i = 0; i < 2; ++i) c.sx[i] = (tile * 128 + 64 * i + 4 * (w >> 1) + 32 * th) * XROW;
  c.vw = r8 * XROW + swz8;
  c.sw = (grp * 128 + 8 * w) * XROW;
  const unsigned tl = lane & 15, q = lane >> 4;
  c.rx = (wm * 64 + tl) * 128 + ((q ^ (tl >> 1)) * 16);
  c.rw0 = (wn * 32 + tl) * 128 + ((q ^ (tl >> 1)) * 16); c.rw1 = c.rw0;
  const unsigned ch_t = (w & 1) * 64 + lane;
  unsigned epoch = 0, pr0 = 0, pr1 = 0, pr2 = 0;
  const u32x4 srdXC = make_srd(a.ws + WS_XCH, 4096u);
  asm volatile("s_nop 4" ::: "memory");
  if (w < 2) {
    const u32x4 s0 = make_srd(a.lre, P_ * 2), s1 = make_srd(a.lim, P_ * 2), s2 = make_srd(a.logdt, P_ * 2);
    asm volatile("buffer_load_dword %0, %1, %2, %3 offen sc1" : "=v"(epoch) : "v"(0), "s"(srdXC), "s"(grp * 256u) : "memory");
    asm volatile("buffer_load_ushort %0, %1, %2, %3 offen" : "=v"(pr0) : "v"(ch_t * 2), "s"(s0), "s"(grp * 256u) : "memory");
    asm volatile("buffer_load_ushort %0, %1, %2, %3 offen" : "=v"(pr1) : "v"(ch_t * 2), "s"(s1), "s"(grp * 256u) : "memory");
    asm volatile("buffer_load_ushort %0, %1, %2, %3 offen" : "=v"(pr2) : "v"(ch_t * 2), "s"(s2), "s"(grp * 256u) : "memory");
  }

  prologue_loads<V>(c);
  if (w < 2) {
    asm volatile("s_waitcnt vmcnt(%c4)" : "+v"(epoch), "+v"(pr0), "+v"(pr1), "+v"(pr2) : "i"(n_prologue<V>()) : "memory");
    const Params p = ssm_params(bf_lo(pr0), bf_lo(pr1), bf_lo(pr2));
    f32x2 pw = {p.lr, p.li};
    *(LAS f32x2*)(lds + SS_PRM + ch_t * 8) = pw;
#pragma unroll
    for (int i = 0; i < 4; ++i) pw = cmul(pw, pw);
    *(LAS f32x2*)(lds + SS_POW + ch_t * 16) = pw;
#pragma unroll
    for (int i = 0; i < 3; ++i) pw = cmul(pw, pw);
    *(LAS f32x2*)(lds + SS_POW + ch_t * 16 + 8) = pw;
    asm volatile("s_waitcnt lgkmcnt(0)" ::: "memory");
    epoch += epoch == 0xffffffffu ? 2u : 1u;
  }
  Frags fa, fb;
  mainloop<V>(c, fa, fb);
  Scan sc;
  const unsigned seg = 4 * wm + q;
#pragma unroll
  for (int cn = 0; cn < 2; ++cn) sc.lam[cn] = *(LAS const f32x2*)(lds + SS_PRM + (32 * wn + 16 * cn + tl) * 8);
  ssm_last(c, fa, fb, sc, std::make_integer_sequence<int, 32>{});
  asm volatile("s_nop 3" ::: "memory");
  scan_piece<2, 0>(c, sc); scan_piece<2, 1>(c, sc); scan_piece<2, 2>(c, sc); scan_piece<2, 3>(c, sc);
  scan_piece<3, 0>(c, sc); scan_piece<3, 1>(c, sc); scan_piece<3, 2>(c, sc); scan_piece<3, 3>(c, sc);
  f32x2 lam[2] = {sc.lam[0], sc.lam[1]};
#pragma unroll
  for (int cn = 0; cn < 2; ++cn) *(LAS f32x2*)(lds + SS_E + (seg * 128 + 32 * wn + 16 * cn + tl) * 8) = sc.e[cn];
  asm volatile("s_waitcnt lgkmcnt(0)\n\ts_barrier" ::: "memory");
  s16x8 S0, S1;
  if (w >= 2) { make_sel(S0, S1, lane); asm volatile("" : "+v"(S0), "+v"(S1)); }
  if (w < 2) {
    const f32x2 l16 = *(LAS const f32x2*)(lds + SS_POW + ch_t * 16), l128 = *(LAS const f32x2*)(lds + SS_POW + ch_t * 16 + 8);
    f32x2 P[9]; P[0] = (f32x2){0.f, 0.f};
    P[1] = *(LAS const f32x2*)(lds + SS_E + ch_t * 8);
#pragma unroll
    for (int sg = 1; sg < 8; ++sg) P[sg + 1] = cfma(l16, P[sg], *(LAS const f32x2*)(lds + SS_E + (sg * 128 + ch_t) * 8));
    const __amdgpu_buffer_rsrc_t rX = __builtin_amdgcn_make_buffer_rsrc(a.ws + WS_XCH, 0, XCH_BYTES, 0x00020000);
    const unsigned gbase = 4096u + grp * 16u * 2048u;
    if (tile < 15) {
      const u32x4 gr = {__float_as_uint(P[8][0]), epoch, __float_as_uint(P[8][1]), epoch};
      store16(gr, rX, ch_t * 16 + gbase + tile * 2048u, 16);
    }
    make_sel(S0, S1, lane); asm volatile("" : "+v"(S0), "+v"(S1));
    f32x2 pw16[8]; pw16[1] = l16;
#pragma unroll
    for (int sg = 2; sg < 8; ++sg) pw16[sg] = cmul(pw16[sg - 1], l16);
    f32x2 carry = {0.f, 0.f};
    if (tile > 0) {
      const u32x4 srdg = make_srd(a.ws + WS_XCH + gbase, 16u * 2048u);
      u32x4 gk[15];
      for (unsigned spins = 0;; ++spins) {
#pragma unroll
        for (int j = 0; j < 15; ++j)
          asm volatile("buffer_load_dwordx4 %0, %1, %2, %3 offen sc1" : "=v"(gk[j]) : "v"(ch_t * 16), "s"(srdg), "s"((unsigned)j < tile ? (tile - 1 - j) * 2048u : 0x40000000u) : "memory");
        asm volatile("s_waitcnt vmcnt(0)" : "+v"(gk[0]), "+v"(gk[1]), "+v"(gk[2]), "+v"(gk[3]), "+v"(gk[4]), "+v"(gk[5]), "+v"(gk[6]), "+v"(gk[7]),
                     "+v"(gk[8]), "+v"(gk[9]), "+v"(gk[10]), "+v"(gk[11]), "+v"(gk[12]), "+v"(gk[13]), "+v"(gk[14]) :: "memory");
        unsigned cnt = 0;
#pragma unroll
        for (int j = 0; j < 15; ++j) cnt += (gk[j][1] == epoch) & (gk[j][3] == epoch);
        if (__all(cnt == tile) || spins > (1u << 20)) break;
        __builtin_amdgcn_s_sleep(1);
      }
#pragma unroll
      for (int j = 14; j >= 0; --j) carry = cfma(l128, carry, (f32x2){__uint_as_float(gk[j][0]), __uint_as_float(gk[j][2])});
    }
    *(LAS f32x2*)(lds + SS_X + ch_t * 8) = carry;
#pragma unroll
    for (int sg = 1; sg < 8; ++sg) *(LAS f32x2*)(lds + SS_X + (sg * 128 + ch_t) * 8) = cfma(pw16[sg], carry, P[sg]);
  }
  asm volatile("s_waitcnt lgkmcnt(0)\n\ts_barrier" ::: "memory");
  if (tile == 15 && w == 0 && lane == 0) {
    __hip_atomic_store((unsigned*)(a.ws + WS_XCH + grp * 256u), epoch, __ATOMIC_RELAXED, __HIP_MEMORY_SCOPE_AGENT);
  }
  const __amdgpu_buffer_rsrc_t rA = __builtin_amdgcn_make_buffer_rsrc(a.ws + WS_AF, 0, AF_BYTES, 0x00020000);
  f32x2 h[2];
#pragma unroll
  for (int cn = 0; cn < 2; ++cn) h[cn] = *(LAS const f32x2*)(lds + SS_X + (seg * 128 + 32 * wn + 16 * cn + tl) * 8);
#pragma unroll
  for (int hh = 0; hh < 2; ++hh) {
    float hr[2][8], hi[2][8];
#pragma unroll
    for (int j = 0; j < 8; ++j)
#pragma unroll
      for (int cn = 0; cn < 2; ++cn) {
        const int i = 8 * hh + j;
        h[cn] = cfma(lam[cn], h[cn], (f32x2){c.acc[i >> 2][2 * cn][i & 3], c.acc[i >> 2][2 * cn + 1][i & 3]});
        hr[cn][j] = h[cn][0]; hi[cn][j] = h[cn][1];
      }
#pragma unroll
    for (int cn = 0; cn < 2; ++cn) {
      const unsigned ks = grp * 8 + wn * 2 + cn;
      const u32x4 ar = {cvtpk(hr[cn][0], hr[cn][1]), cvtpk(hr[cn][2], hr[cn][3]), cvtpk(hr[cn][4], hr[cn][5]), cvtpk(hr[cn][6], hr[cn][7])};
      const u32x4 ai = {cvtpk(hi[cn][0], hi[cn][1]), cvtpk(hi[cn][2], hi[cn][3]), cvtpk(hi[cn][4], hi[cn][5]), cvtpk(hi[cn][6], hi[cn][7])};
#pragma unroll
      for (int Q = 0; Q < 2; ++Q) {
        const f32x4 dr = mfma_sel(ar, Q ? S1 : S0), di = mfma_sel(ai, Q ? S1 : S0);
        const u32x4 o = {cvtpk(dr[0], dr[1]), cvtpk(dr[2], dr[3]), cvtpk(di[0], di[1]), cvtpk(di[2], di[3])};
        store16(o, rA, lane * 16 + ((tile * KSTEPS + ks) * 8 + 4 * wm + 2 * Q + hh) * 1024u, WT);
      }
    }
  }
}

__global__ __launch_bounds__(512, 2) void k1_kernel(const K1Args a, const K1Map map_arg) {
  extern __shared__ __attribute__((aligned(16))) char lds_raw[];
  LAS char* lds = (LAS char*)lds_raw;
  const int lane = threadIdx.x & 63;
  const unsigned w = __builtin_amdgcn_readfirstlane(threadIdx.x >> 6);
  code_touch(blockIdx.x >> 3, w, lane);
  const __attribute__((address_space(4))) unsigned* kmap = (const __attribute__((address_space(4))) unsigned*)((const __attribute__((address_space(4))) char*)__builtin_amdgcn_kernarg_segment_ptr() + sizeof(K1Args));
  const unsigned role = kmap[blockIdx.x];
  const unsigned type = (role >> 12) & 15, grp = (role >> 8) & 15, tile = role & 255;
  const unsigned sjob = (role >> 16) * 8u + w;
  if (__builtin_expect(type == k1::V_T1, 1)) ffn_body<k1::V_T1>(a, lds, w, lane, tile, grp, sjob);
  else if (type == k1::V_SSM) ssm_body<0>(a, lds, w, lane, tile, grp);
  else ffn_body<k1::V_T5>(a, lds, w, lane, tile, 0, sjob);
}


static void k1_build_map(unsigned* out) {
  unsigned short map[256]; unsigned char fmap[256];
  for (int b = 0; b < 256; ++b) {
    const int xcd = b & 7, slot = b >> 3;
    if (slot < 10) { const int i = xcd * 10 + slot; map[b] = (unsigned short)(k1::V_SSM << 12 | (i >> 4) << 8 | (i & 15)); fmap[b] = 0; }
    else { const int i = xcd * 22 + slot - 10; fmap[b] = (unsigned char)i;
      map[b] = (unsigned short)(i < 169 ? (k1::V_T1 << 12 | (i / 13) << 8 | (i % 13)) : (k1::V_T5 << 12 | (i - 169))); }
  }
  for (int b = 0; b < 256; ++b) out[b] = (unsigned)fmap[b] << 16 | map[b];
}

namespace k2 {
constexpr int NSLOT = 3;
constexpr int SLOTR = 52;
constexpr int RED_W = 30720;
constexpr int XS_OFF = 4 * RED_W, XS_W = 6144, LDS_K2 = XS_OFF + 4 * XS_W;
constexpr int TP = 1088;

#define K2_ACLOB "a0","a1","a2","a3","a4","a5","a6","a7","a8","a9","a10","a11","a12","a13","a14","a15","a16","a17","a18","a19","a20","a21","a22","a23","a24","a25","a26","a27","a28","a29","a30","a31","a32","a33","a34","a35","a36","a37","a38","a39","a40","a41","a42","a43","a44","a45","a46","a47","a48","a49","a50","a51","a52","a53","a54","a55","a56","a57","a58","a59","a60","a61","a62","a63","a64","a65","a66","a67","a68","a69","a70","a71","a72","a73","a74","a75","a76","a77","a78","a79","a80","a81","a82","a83","a84","a85","a86","a87","a88","a89","a90","a91","a92","a93","a94","a95","a96","a97","a98","a99","a100","a101","a102","a103","a104","a105","a106","a107","a108","a109","a110","a111","a112","a113","a114","a115","a116","a117","a118","a119","a120","a121","a122","a123","a124","a125","a126","a127","a128","a129","a130","a131","a132","a133","a134","a135","a136","a137","a138","a139","a140","a141","a142","a143","a144","a145","a146","a147","a148","a149","a150","a151","a152","a153","a154","a155"

template <int ROLE> struct RC;
template <> struct RC<0> { static constexpr int NSTEP = 10, XSTEP = 1; };
template <> struct RC<1> { static constexpr int NSTEP = 14, XSTEP = 99; };

struct Ctx {
  u32x4 srdA, srdW, srdX, srdD, srdH;
  unsigned va[8];
  unsigned vw0, vw4;
  unsigned abase, wbase;
  bool full;
  unsigned xm0, tok0, xcol;
  u32x2 xv, dv;
  LAS char *xs, *ds, *red;
  unsigned w;
  f32x4 acc[8][5];
};

template <int ROLE, int STEP> DI void step_soff(Ctx& c, unsigned& sa, unsigned& sw) {
  sa = (c.abase + STEP) * 8192u;
  sw = (c.wbase + STEP) * 5120u;
  if constexpr (ROLE == 1 && STEP == RC<1>::NSTEP - 1) { sa = c.full ? sa : 0x40000000u; sw = c.full ? sw : 0x40000000u; }
}
template <int STEP, int I> DI void ld(Ctx& c, unsigned sa, unsigned sw) {
  constexpr int r = (STEP % NSLOT) * SLOTR + 4 * I;
  if constexpr (I < 8) asm volatile("buffer_load_dwordx4 a[%c0:%c1], %2, %3, %4 offen" :: "i"(r), "i"(r + 3), "v"(c.va[I]), "s"(c.srdA), "s"(sa) : "memory");
  else if constexpr (I < 12) asm volatile("buffer_load_dwordx4 a[%c0:%c1], %2, %3, %4 offen offset:%c5" :: "i"(r), "i"(r + 3), "v"(c.vw0), "s"(c.srdW), "s"(sw), "i"((I - 8) * 1024) : "memory");
  else asm volatile("buffer_load_dwordx4 a[%c0:%c1], %2, %3, %4 offen" :: "i"(r), "i"(r + 3), "v"(c.vw4), "s"(c.srdW), "s"(sw) : "memory");
}
template <int STEP, int M, int N> DI void mm(Ctx& c) {
  constexpr int base = (STEP % NSLOT) * SLOTR, ra = base + 4 * M, rw = base + 32 + 4 * N;
  if constexpr (STEP == 0) asm volatile("v_mfma_f32_16x16x32_bf16 %0, a[%c1:%c2], a[%c3:%c4], 0" : "=v"(c.acc[M][N]) : "i"(rw), "i"(rw + 3), "i"(ra), "i"(ra + 3) : "memory");
  else asm volatile("v_mfma_f32_16x16x32_bf16 %0, a[%c1:%c2], a[%c3:%c4], %0" : "+v"(c.acc[M][N]) : "i"(rw), "i"(rw + 3), "i"(ra), "i"(ra + 3) : "memory");
}
template <int N> DI void waitv() { asm volatile("s_waitcnt vmcnt(%c0)" :: "i"(N) : "memory"); }

template <int ROLE> constexpr int n_extra() { return ROLE == 0 ? 6 : 0; }
template <int ROLE> DI void extra_loads(Ctx& c) {
  if constexpr (ROLE == 0) {
    unsigned lane = c.vw0 >> 4; asm volatile("" : "+v"(lane));
#pragma unroll
    for (int i = 0; i < 5; ++i) {
      const unsigned p = i * 64 + lane, r = p / 10, ch = p % 10;
      const unsigned off = (c.tok0 + 8 * (r >> 4) + 16 * ((r >> 3) & 1) + (r & 7)) * 1280u + c.xcol + ch * 16u;
      asm volatile("s_mov_b32 m0, %0\n\ts_nop 0\n\tbuffer_load_dwordx4 %1, %2, 0 offen lds" :: "s"(c.xm0 + i * 1024u), "v"(off), "s"(c.srdX) : "memory");
    }
    const unsigned dof = lane < 10 ? c.xcol + lane * 16u : 0x40000000u;
    asm volatile("s_mov_b32 m0, %0\n\ts_nop 0\n\tbuffer_load_dwordx4 %1, %2, 0 offen lds" :: "s"(c.xm0 + 5120u), "v"(dof), "s"(c.srdD) : "memory");
  }
}
template <int J, int N> DI void skip_read(Ctx& c) { c.xv = *(LAS const u32x2*)(c.xs + J * 2560 + N * 32); c.dv = *(LAS const u32x2*)(c.ds + N * 32); }
template <int J, int N> DI void skip_term(Ctx& c) {
  const u32x2 xv = c.xv, dv = c.dv;
  asm volatile("" : "+v"(c.acc[J][N]));
  float v0 = c.acc[J][N][0], v1 = c.acc[J][N][1], v2 = c.acc[J][N][2], v3 = c.acc[J][N][3];
  unsigned t0, t1, t2, t3;
  asm volatile("v_and_b32 %4, 0xffff, %10\n\tv_and_b32 %5, 0xffff0000, %10\n\tv_and_b32 %6, 0xffff, %11\n\tv_and_b32 %7, 0xffff0000, %11\n\t"
               "v_dot2c_f32_bf16 %0, %8, %4\n\tv_dot2c_f32_bf16 %1, %8, %5\n\tv_dot2c_f32_bf16 %2, %9, %6\n\tv_dot2c_f32_bf16 %3, %9, %7"
               : "+v"(v0), "+v"(v1), "+v"(v2), "+v"(v3), "=&v"(t0), "=&v"(t1), "=&v"(t2), "=&v"(t3)
               : "v"(xv[0]), "v"(xv[1]), "v"(dv[0]), "v"(dv[1]));
  c.acc[J][N] = (f32x4){v0, v1, v2, v3};
  asm volatile("s_nop 2" : "+v"(c.acc[J][N]));
}
template <int ROLE, int STEP, int I> DI void item(Ctx& c, unsigned sa, unsigned sw) {
  mm<STEP, I / 5, I % 5>(c);
  if constexpr (STEP + NSLOT - 1 < RC<ROLE>::NSTEP && I % 3 == 1 && I / 3 < 13) ld<STEP + NSLOT - 1, I / 3>(c, sa, sw);
  if constexpr (ROLE == 0 && STEP >= 4 && STEP <= 7) {
    constexpr int t0 = STEP < 6 ? 3 * (STEP - 4) : 6 + 2 * (STEP - 6), nt = STEP < 6 ? 3 : 2;
    if constexpr (I >= 15 && (I - 15) % 7 == 0 && (I - 15) / 7 < nt) skip_read<(t0 + (I - 15) / 7) / 5, (t0 + (I - 15) / 7) % 5>(c);
    if constexpr (I >= 20 && (I - 20) % 7 == 0 && (I - 20) / 7 < nt) skip_term<(t0 + (I - 20) / 7) / 5, (t0 + (I - 20) / 7) % 5>(c);
  }
}
template <int ROLE, int STEP, int... I> DI void step_items(Ctx& c, unsigned sa, unsigned sw, std::integer_sequence<int, I...>) { (item<ROLE, STEP, I>(c, sa, sw), ...); }
template <int ROLE, int STEP> DI void step(Ctx& c) {
  constexpr int NSTEP = RC<ROLE>::NSTEP, XSTEP = RC<ROLE>::XSTEP, LS = STEP + NSLOT - 1;
  unsigned sa = 0, sw = 0;
  if constexpr (LS < NSTEP) step_soff<ROLE, LS>(c, sa, sw);
  constexpr int last_issued = (STEP + NSLOT - 2 < NSTEP - 1) ? STEP + NSLOT - 2 : NSTEP - 1;
  constexpr int extra = (STEP > XSTEP && STEP <= XSTEP + NSLOT - 1) ? n_extra<ROLE>() : 0;
  waitv<13 * (last_issued - STEP) + extra>();
  step_items<ROLE, STEP>(c, sa, sw, std::make_integer_sequence<int, 40>{});
  if constexpr (STEP == XSTEP) extra_loads<ROLE>(c);
}
template <int STEP, int... I> DI void pro_items(Ctx& c, unsigned sa, unsigned sw, std::integer_sequence<int, I...>) { (ld<STEP, I>(c, sa, sw), ...); }
template <int ROLE, int STEP> DI void prologue_loads(Ctx& c) {
  unsigned sa, sw; step_soff<ROLE, STEP>(c, sa, sw);
  pro_items<STEP>(c, sa, sw, std::integer_sequence<int, 0, 8, 9, 10, 11, 12, 1, 2, 3, 4, 5, 6, 7>{});
}
template <int ROLE, int... S> DI void steps(Ctx& c, std::integer_sequence<int, S...>) { (step<ROLE, S>(c), ...); }
template <int I, int N> DI void red_write(Ctx& c) {
  asm volatile("" : "+v"(c.acc[I][N]));
  *(LAS f32x4*)(c.red + ((c.w + (I >> 1)) & 3) * RED_W + ((I >> 1) - 1) * 10240 + (I & 1) * 5120 + N * 1024) = c.acc[I][N];
}
template <int ROLE, int I> DI void tail_item(Ctx& c) {
  constexpr int g = I / 10, k = I % 10, m = (g + 2) & 7, NSTEP = RC<ROLE>::NSTEP;
  mm<NSTEP - 2 + k / 5, m, k % 5>(c);
  if constexpr (g >= 1 && g <= 6 && k >= 4 && k <= 8) red_write<g + 1, k - 4>(c);
}
template <int ROLE, int... I> DI void tail_items(Ctx& c, std::integer_sequence<int, I...>) { (tail_item<ROLE, I>(c), ...); }
template <int ROLE> DI void tail(Ctx& c) {
  waitv<0>();
  tail_items<ROLE>(c, std::make_integer_sequence<int, 80>{});
}
}

struct K2Args {
  const unsigned *AF, *WF; const float* gate; const u16 *x, *dskip; float* out; char* ho;
};

DI void partner_loads(u32x4 (&pt)[2][5], const unsigned poff, const u32x4 srdP) {
#pragma unroll
  for (int j = 0; j < 2; ++j)
#pragma unroll
    for (int n = 0; n < 5; ++n) asm volatile("buffer_load_dwordx4 %0, %1, %2, %3 offen sc1" : "=v"(pt[j][n]) : "v"(poff), "s"(srdP), "s"((j * 5 + n) * 1024u) : "memory");
}
DI bool partner_valid(const u32x4 (&pt)[2][5], const unsigned tlo, const unsigned thi) {
  unsigned bad = 0;
#pragma unroll
  for (int j = 0; j < 2; ++j)
#pragma unroll
    for (int n = 0; n < 5; ++n) bad |= ((pt[j][n][0] ^ tlo) | (pt[j][n][1] ^ thi) | (pt[j][n][2] ^ tlo) | (pt[j][n][3] ^ thi)) & 0xfffu;
  return __all(bad == 0);
}
template <int ROLE> DI void k2_body(const K2Args& a, LAS char* lds, const int w, const int lane, const unsigned tt, const unsigned dg) {
  using namespace k2;
  const int tl = lane & 15, q = lane >> 4;
  const unsigned pair = tt * 8 + dg;
  Ctx c;
  c.srdA = make_srd(a.AF, AF_BYTES); c.srdW = make_srd(a.WF, WF_BYTES); c.srdH = make_srd(a.ho, HO_WORDS);
  c.srdX = make_srd(a.x, 2048 * 640 * 2); c.srdD = make_srd(a.dskip, 640 * 2);
  const u32x4 srdG = make_srd(a.gate, 2048 * 4);
#pragma unroll
  for (int i = 0; i < 8; ++i) c.va[i] = lane * 16 + ((i + 2 * w) & 7) * 1024;
  c.vw0 = lane * 16; c.vw4 = lane * 16 + 4096;
  const unsigned k0 = ROLE == 0 ? 10 * w : 40 + (w < 2 ? 14 * w : 28 + 13 * (w - 2));
  c.abase = tt * KSTEPS + k0; c.wbase = dg * KSTEPS + k0; c.full = w < 2;
  c.tok0 = 128 * tt + 64 * (w >> 1) + 32 * (w & 1); c.xcol = 160u * dg; c.xm0 = XS_OFF + w * XS_W;
  c.xs = lds + c.xm0 + tl * 160 + q * 8; c.ds = lds + c.xm0 + 5120 + q * 8; c.red = lds + lane * 16; c.w = w;
  unsigned graw, epoch;
  asm volatile("s_nop 4\n\tbuffer_load_dword %0, %1, %2, %3 offen" : "=v"(graw) : "v"(lane * 4), "s"(srdG), "s"(c.tok0 * 4u) : "memory");
  asm volatile("buffer_load_dword %0, %1, %2, %3 offen sc1" : "=v"(epoch) : "v"(0), "s"(c.srdH), "s"(pair * 256u) : "memory");
  prologue_loads<ROLE, 0>(c); prologue_loads<ROLE, 1>(c);
  static_assert(NSLOT == 3, "prologue");
  asm volatile("s_waitcnt vmcnt(26)" : "+v"(graw), "+v"(epoch) :: "memory", K2_ACLOB);
  float g[2];
#pragma unroll
  for (int j = 0; j < 2; ++j) g[j] = __int_as_float(__builtin_amdgcn_ds_bpermute((8 * j + 16 * (tl >> 3) + (tl & 7)) * 4, (int)graw));
  epoch += 1;
  steps<ROLE>(c, std::make_integer_sequence<int, RC<ROLE>::NSTEP - 2>{});
  unsigned prd[5], pst[5];
  if constexpr (ROLE == 1) {
    unsigned ln = lane; asm volatile("" : "+v"(ln));
#pragma unroll
    for (int i = 0; i < 5; ++i) {
      const unsigned p = i * 64 + ln, r = p / 20, ch = p % 20;
      prd[i] = (ch >> 2) * TP + (ch & 3) * 272 + r * 16;
      pst[i] = (c.tok0 + 16 * (r >> 3) + (r & 7)) * 2560u + 320u * dg + ch * 16u;
      asm volatile("" : "+v"(prd[i]), "+v"(pst[i]));
    }
  }
  tail<ROLE>(c);
  asm volatile("s_nop 7" ::: "memory");
#pragma unroll
  for (int m = 0; m < 2; ++m) asm volatile("" : "+v"(c.acc[m][0]), "+v"(c.acc[m][1]), "+v"(c.acc[m][2]), "+v"(c.acc[m][3]), "+v"(c.acc[m][4]));
  const __amdgpu_buffer_rsrc_t rP = __builtin_amdgcn_make_buffer_rsrc(a.ho + HO_WORDS, 0, HO_TILES, 0x00020000);
  const unsigned poff = (pair * 8 + 2 * w) * 5120u + lane * 16u;
  asm volatile("s_waitcnt lgkmcnt(0)\n\ts_barrier" ::: "memory");
  u32x4 ptile[2][5];
  const u32x4 srdP = make_srd(a.ho + HO_WORDS, HO_TILES);
  const unsigned tlo = epoch & 0xfffu, thi = (epoch >> 12) & 0xfffu;
  if constexpr (ROLE == 1) partner_loads(ptile, poff, srdP);
  f32x4 v[2][5];
  const LAS char* src = lds + w * RED_W + lane * 16;
#pragma unroll
  for (int j = 0; j < 2; ++j) {
#pragma unroll
    for (int n = 0; n < 5; ++n) v[j][n] = (c.acc[j][n] + *(const LAS f32x4*)(src + (j * 5 + n) * 1024)) + (*(const LAS f32x4*)(src + 10240 + (j * 5 + n) * 1024) + *(const LAS f32x4*)(src + 20480 + (j * 5 + n) * 1024));
  }
  if constexpr (ROLE == 0) {
#pragma unroll
    for (int j = 0; j < 2; ++j)
#pragma unroll
      for (int n = 0; n < 5; ++n) {
        const u32x4 b = __builtin_bit_cast(u32x4, v[j][n] * g[j]);
        const u32x4 o = {(b[0] & ~0xfffu) | tlo, (b[1] & ~0xfffu) | thi, (b[2] & ~0xfffu) | tlo, (b[3] & ~0xfffu) | thi};
        store16(o, rP, poff + (j * 5 + n) * 1024u, 16);
      }
  } else {
#pragma unroll
    for (int j = 0; j < 2; ++j) asm volatile("" : "+v"(v[j][0]), "+v"(v[j][1]), "+v"(v[j][2]), "+v"(v[j][3]), "+v"(v[j][4]));
    asm volatile("s_waitcnt vmcnt(0) lgkmcnt(0)" : "+v"(ptile[0][0]), "+v"(ptile[0][1]), "+v"(ptile[0][2]), "+v"(ptile[0][3]), "+v"(ptile[0][4]),
                 "+v"(ptile[1][0]), "+v"(ptile[1][1]), "+v"(ptile[1][2]), "+v"(ptile[1][3]), "+v"(ptile[1][4]) :: "memory");
    for (unsigned spins = 0; !partner_valid(ptile, tlo, thi) && spins < (1u << 20); ++spins) {
      __builtin_amdgcn_s_sleep(1);
      asm volatile("buffer_load_dwordx4 %0, %10, %11, 0 offen sc1\n\tbuffer_load_dwordx4 %1, %10, %11, 0 offen offset:1024 sc1\n\t"
                   "buffer_load_dwordx4 %2, %10, %11, 0 offen offset:2048 sc1\n\tbuffer_load_dwordx4 %3, %10, %11, 0 offen offset:3072 sc1\n\t"
                   "buffer_load_dwordx4 %4, %10, %11, %12 offen sc1\n\tbuffer_load_dwordx4 %5, %10, %11, %12 offen offset:1024 sc1\n\t"
                   "buffer_load_dwordx4 %6, %10, %11, %12 offen offset:2048 sc1\n\tbuffer_load_dwordx4 %7, %10, %11, %12 offen offset:3072 sc1\n\t"
                   "buffer_load_dwordx4 %8, %10, %11, %13 offen sc1\n\tbuffer_load_dwordx4 %9, %10, %11, %13 offen offset:1024 sc1\n\ts_waitcnt vmcnt(0)"
                   : "=&v"(ptile[0][0]), "=&v"(ptile[0][1]), "=&v"(ptile[0][2]), "=&v"(ptile[0][3]), "=&v"(ptile[0][4]),
                     "=&v"(ptile[1][0]), "=&v"(ptile[1][1]), "=&v"(ptile[1][2]), "=&v"(ptile[1][3]), "=&v"(ptile[1][4])
                   : "v"(poff), "s"(srdP), "s"(4096u), "s"(8192u) : "memory");
    }
#pragma unroll
    for (int j = 0; j < 2; ++j)
#pragma unroll
      for (int n = 0; n < 5; ++n) ptile[j][n] &= ~0xfffu;
    LAS char* tr = lds + w * RED_W;
#pragma unroll
    for (int j = 0; j < 2; ++j) {
      const float g1 = rbf(1.f - g[j]);
#pragma unroll
      for (int n = 0; n < 5; ++n) *(LAS f32x4*)(tr + (j * 5 + n) * TP + q * 272 + tl * 16) = __builtin_bit_cast(f32x4, ptile[j][n]) + g1 * v[j][n];
    }
    const __amdgpu_buffer_rsrc_t rO = __builtin_amdgcn_make_buffer_rsrc(a.out, 0, 2048 * 640 * 4, 0x00020000);
#pragma unroll
    for (int j = 0; j < 2; ++j)
#pragma unroll
      for (int i = 0; i < 5; ++i) {
        const f32x4 o = *(const LAS f32x4*)(tr + j * 5 * TP + prd[i]);
        store16(__builtin_bit_cast(u32x4, o), rO, pst[i] + j * 8 * 2560u, WT);
      }
    asm volatile("s_barrier" ::: "memory");
    if (w == 0 && lane == 0) {
      const __amdgpu_buffer_rsrc_t rH = __builtin_amdgcn_make_buffer_rsrc(a.ho, 0, HO_WORDS, 0x00020000);
      __builtin_amdgcn_raw_buffer_store_b32(epoch, rH, pair * 256u, 0, 16);
    }
  }
}

__global__ __launch_bounds__(256, 1) void k2_kernel(const K2Args a) {
  extern __shared__ __attribute__((aligned(16))) char k2_lds[];
  LAS char* lds = (LAS char*)k2_lds;
  const int lane = threadIdx.x & 63;
  const int w = __builtin_amdgcn_readfirstlane(threadIdx.x >> 6);
  const unsigned xcd = blockIdx.x & 7, idx = blockIdx.x >> 3;
  const unsigned tt = (xcd >> 1) * 4 + (idx >> 3), dg = (xcd & 1) * 4 + ((idx >> 1) & 3);
  if (idx & 1) k2_body<1>(a, lds, w, lane, tt, dg); else k2_body<0>(a, lds, w, lane, tt, dg);
}

extern "C" __global__ void zz_code_end() {}
extern "C" void kernel_launch(void* const* d_in, const int* in_sizes, int n_in, void* d_out, int out_size, void* d_ws, size_t ws_size, hipStream_t stream) {
  K1Args a;
  a.x = (const u16*)d_in[0]; a.lre = (const u16*)d_in[1]; a.lim = (const u16*)d_in[2]; a.bre = (const u16*)d_in[3]; a.bim = (const u16*)d_in[4];
  a.cre = (const u16*)d_in[5]; a.cim = (const u16*)d_in[6]; a.logdt = (const u16*)d_in[7];
  a.wg = (const u16*)d_in[10]; a.wu = (const u16*)d_in[11]; a.wd = (const u16*)d_in[12]; a.gw = (const u16*)d_in[13];
  a.ws = (char*)d_ws;
  K1Map map; k1_build_map(map.v);
  (void)hipFuncSetAttribute((const void*)k1_kernel, hipFuncAttributeMaxDynamicSharedMemorySize, k1::LDS_BYTES);
  k1_kernel<<<256, 512, k1::LDS_BYTES, stream>>>(a, map);
  char* ws = (char*)d_ws;
  K2Args b;
  b.AF = (const unsigned*)(ws + WS_AF); b.WF = (const unsigned*)(ws + WS_WF); b.gate = (const float*)(ws + WS_GATE);
  b.x = (const u16*)d_in[0]; b.dskip = (const u16*)d_in[8]; b.out = (float*)d_out; b.ho = ws + WS_HO;
  (void)hipFuncSetAttribute((const void*)k2_kernel, hipFuncAttributeMaxDynamicSharedMemorySize, k2::LDS_K2);
  k2_kernel<<<256, 256, k2::LDS_K2, stream>>>(b);
}
```

```cpp
#include <hip/hip_runtime.h>
#include <stdint.h>
#include <utility>

typedef unsigned short u16;
typedef float f32x4 __attribute__((ext_vector_type(4)));
typedef float f32x2 __attribute__((ext_vector_type(2)));
typedef unsigned u32x4 __attribute__((ext_vector_type(4)));
typedef unsigned u32x2 __attribute__((ext_vector_type(2)));
typedef short s16x4 __attribute__((ext_vector_type(4)));
typedef short s16x8 __attribute__((ext_vector_type(8)));
typedef __bf16 bf16x2 __attribute__((ext_vector_type(2)));
#define DI __device__ __forceinline__
#define LAS __attribute__((address_space(3)))

constexpr int L_ = 2048, D_ = 640, P_ = 640, F_ = 1728;
constexpr int KSTEPS = 94;
constexpr unsigned AF_BYTES = 16u * KSTEPS * 8192u;
constexpr unsigned WF_BYTES = 8u * KSTEPS * 5120u;
constexpr size_t WS_AF = 0, WS_WF = AF_BYTES, WS_GATE = WS_WF + WF_BYTES, WS_XCH = WS_GATE + 8192;
constexpr unsigned XCH_BYTES = 4096u + 5u * 16u * 2048u;
constexpr unsigned HO_WORDS = 128u * 256u, HO_TILES = 128u * 40960u;
constexpr size_t WS_HO = WS_XCH + XCH_BYTES;
constexpr int WT = 16;

DI float bf_lo(unsigned v) { return __uint_as_float(v << 16); }
DI float bf_hi(unsigned v) { return __uint_as_float(v & 0xffff0000u); }
DI float bf2f(u16 v) { return __uint_as_float(((unsigned)v) << 16); }
DI unsigned cvtpk(float lo, float hi) { f32x2 v = {lo, hi}; bf16x2 b = __builtin_convertvector(v, bf16x2); return __builtin_bit_cast(unsigned, b); }
DI float rbf(float f) { return bf_lo(cvtpk(f, f)); }
DI u32x4 make_srd(const void* p, unsigned bytes) {
  const unsigned long long a = (unsigned long long)p;
  u32x4 r; r[0] = (unsigned)a; r[1] = (unsigned)(a >> 32) & 0xffffu; r[2] = bytes; r[3] = 0x00020000u; return r;
}

#define store16(data, rsrc, voff, aux) __builtin_amdgcn_raw_buffer_store_b128((data), (rsrc), (voff), 0, (aux))

struct K1Args;
namespace k1 {
enum { V_T1 = 0, V_T5 = 1, V_SSM = 2 };
constexpr int LDS_BYTES = 163840;
constexpr int NSTAGE = 10;
constexpr unsigned XROW = 1280, WROW = 3456;
constexpr int SS_E = 147456, SS_PRM = SS_E + 8192, SS_POW = SS_PRM + 1024, SS_X = 0;

template <int V> struct Cfg;
template <> struct Cfg<V_T1>  { static constexpr int NX = 5, XS = 3, WS = 3, XB = 20480, WB = 32768, XSTR = 53248, WSTR = 53248, WOFF = 20480, NLX = 3, NLW = 4; };
template <> struct Cfg<V_T5>  { static constexpr int NX = 5, XS = 2, WS = 3, XB = 40960, WB = 16384, XSTR = 40960, WSTR = 16384, WOFF = 81920, NLX = 5, NLW = 2; };
template <> struct Cfg<V_SSM> { static constexpr int NX = 4, XS = 3, WS = 3, XB = 16384, WB = 32768, XSTR = 49152, WSTR = 49152, WOFF = 16384, NLX = 2, NLW = 4; };

struct Side {
  u32x4 xa[2], ga; unsigned xb[2], gb, pr[3];
  u32x4 srdX, srdG, srdA, srdB, srdP[3];
  unsigned v16, v4, vw, vp, sx[2], sw[4], sp, m0;
};
template <int I> DI void side_ld(Side& s);
constexpr int NSIDE = 13;
struct Ctx {
  u32x4 srdX, srdW0, srdW1;
  unsigned vx, vx3, vw;
  unsigned sx[5];
  unsigned sx3, sw;
  unsigned w;
  unsigned rx, rw0, rw1;
  LAS char* lds;
  f32x4 acc[5][4];
  Side* side;
};

DI void glds(unsigned m0, unsigned voff, u32x4 srd, unsigned soff) {
  asm volatile("s_mov_b32 m0, %0\n\ts_nop 0\n\tbuffer_load_dwordx4 %1, %2, %3 offen lds" :: "s"(m0), "v"(voff), "s"(srd), "s"(soff) : "memory");
}
DI void glds_half(unsigned m0, unsigned voff, u32x4 srd, unsigned soff) {
  asm volatile("s_mov_b32 m0, %0\n\ts_mov_b32 exec_hi, 0\n\tbuffer_load_dwordx4 %1, %2, %3 offen lds\n\ts_mov_b32 exec_hi, -1" :: "s"(m0), "v"(voff), "s"(srd), "s"(soff) : "memory");
}
template <int N> DI void waitv() { asm volatile("s_waitcnt vmcnt(%c0)" :: "i"(N) : "memory"); }

template <int V, int S, int I> DI void ldx(Ctx& c) {
  using C = Cfg<V>;
  constexpr unsigned slot = (S % C::XS) * C::XSTR;
  if constexpr (V == V_T1 && I == 2) glds_half(slot + 16384u + c.w * 512u, c.vx3, c.srdX, c.sx3 + S * 128u);
  else glds(slot + (c.w + 8u * I) * 1024u, c.vx, c.srdX, c.sx[I] + S * 128u);
}
template <int V, int S, int I> DI void ldw(Ctx& c) {
  using C = Cfg<V>;
  constexpr unsigned slot = C::WOFF + (S % C::WS) * C::WSTR;
  if constexpr (V == V_T1) {
    glds(slot + (I >> 1) * 16384u + (2u * c.w + (I & 1)) * 1024u, c.vw, (I >> 1) ? c.srdW1 : c.srdW0, c.sw + (S * 64u + (I & 1) * 4u) * WROW);
  } else if constexpr (V == V_T5) {
    glds(slot + I * 8192u + c.w * 1024u, c.vw, I ? c.srdW1 : c.srdW0, c.sw + S * 64u * WROW);
  } else {
    glds(slot + (I >> 1) * 16384u + (c.w + 8u * (I & 1)) * 1024u, c.vw, (I >> 1) ? c.srdW1 : c.srdW0, c.sw + (I & 1) * 64u * XROW + S * 128u);
  }
}
struct Frags { s16x8 x[5]; s16x8 w[4]; s16x4 wl[4], wh[4]; };

template <int V, int S, int KK, int I> DI void rd(Ctx& c, Frags& f) {
  using C = Cfg<V>;
  if constexpr (I < C::NX) {
    constexpr unsigned off = (S % C::XS) * C::XSTR + I * 2048u;
    f.x[I] = *(LAS const s16x8*)(c.lds + ((c.rx ^ (KK * 64u)) + off));
  } else if constexpr (V == V_SSM) {
    constexpr int n = I - C::NX;
    constexpr unsigned off = C::WOFF + (S % C::WS) * C::WSTR + (n & 1) * 16384u + (n >> 1) * 2048u;
    f.w[n] = *(LAS const s16x8*)(c.lds + ((c.rw0 ^ (KK * 64u)) + off));
  } else {
    constexpr int j = I - C::NX, n = j >> 1, half = j & 1;
    constexpr unsigned rowb = (V == V_T1) ? 256u : 128u, matb = (V == V_T1) ? 16384u : 8192u;
    constexpr unsigned off = C::WOFF + (S % C::WS) * C::WSTR + (n & 1) * matb + (KK * 32u + half * 4u) * rowb;
    const s16x4 v = __builtin_amdgcn_ds_read_tr16_b64_v4i16((LAS s16x4*)(c.lds + (((n >> 1) ? c.rw1 : c.rw0) + off)));
    if constexpr (half == 0) f.wl[n] = v; else { f.wh[n] = v; f.w[n] = __builtin_shufflevector(f.wl[n], v, 0, 1, 2, 3, 4, 5, 6, 7); }
  }
}
template <int V> constexpr int nreads() { return V == V_SSM ? 8 : 13; }
template <int V> constexpr int nmm() { return Cfg<V>::NX * 4; }
template <int V, bool FIRST, int I> DI void mm(Ctx& c, Frags& f) {
  constexpr int m = I % Cfg<V>::NX, n = I / Cfg<V>::NX;
  if constexpr (V == V_SSM) {
    if constexpr (FIRST) asm volatile("v_mfma_f32_16x16x32_bf16 %0, %1, %2, 0" : "=v"(c.acc[m][n]) : "v"(f.x[m]), "v"(f.w[n]) : "memory");
    else asm volatile("v_mfma_f32_16x16x32_bf16 %0, %1, %2, %0" : "+v"(c.acc[m][n]) : "v"(f.x[m]), "v"(f.w[n]) : "memory");
  } else {
    if constexpr (FIRST) asm volatile("v_mfma_f32_16x16x32_bf16 %0, %1, %2, 0" : "=v"(c.acc[m][n]) : "v"(f.w[n]), "v"(f.x[m]) : "memory");
    else asm volatile("v_mfma_f32_16x16x32_bf16 %0, %1, %2, %0" : "+v"(c.acc[m][n]) : "v"(f.w[n]), "v"(f.x[m]) : "memory");
  }
}
template <int V, int S, int I> DI void half0_item(Ctx& c, Frags& fa, Frags& fb) {
  using C = Cfg<V>;
  mm<V, S == 0, I>(c, fa);
  if constexpr (I < nreads<V>()) rd<V, S, 1, I>(c, fb);
  if constexpr (S == 0) {
    constexpr int NX2 = (C::XS == 3) ? C::NLX : 0, J = I - (nmm<V>() - NX2 - C::NLW);
    if constexpr (J >= 0 && J < NX2) ldx<V, 2, J>(c);
    else if constexpr (J >= NX2) ldw<V, 2, J - NX2>(c);
  }
}
template <int V, int S, int... I> DI void half0(Ctx& c, Frags& fa, Frags& fb, std::integer_sequence<int, I...>) { (half0_item<V, S, I>(c, fa, fb), ...); }
template <int V, int S, int J> DI void stage_ld(Ctx& c) {
  using C = Cfg<V>;
  if constexpr (J < C::NLX) { if constexpr (S + C::XS < NSTAGE) ldx<V, S + C::XS, J>(c); }
  else if constexpr (J < C::NLX + C::NLW) { if constexpr (S + C::WS < NSTAGE) ldw<V, S + C::WS, J - C::NLX>(c); }
}
template <int V, int S, int I> DI void half1_item(Ctx& c, Frags& fa, Frags& fb) {
  mm<V, false, I>(c, fb);
  if constexpr (S + 1 < NSTAGE && I < nreads<V>()) rd<V, S + 1, 0, I>(c, fa);
  constexpr int J = I - (nmm<V>() - (Cfg<V>::NLX + Cfg<V>::NLW));
  if constexpr (J >= 0) stage_ld<V, S, J>(c);
  if constexpr (V != V_SSM && S == 8 && I % 3 == 2 && I / 3 < 6) side_ld<I / 3>(*c.side);
}
template <int V, int S, int... I> DI void half1(Ctx& c, Frags& fa, Frags& fb, std::integer_sequence<int, I...>) { (half1_item<V, S, I>(c, fa, fb), ...); }

template <int V, int S> DI void stage(Ctx& c, Frags& fa, Frags& fb) {
  using C = Cfg<V>;
  half0<V, S>(c, fa, fb, std::make_integer_sequence<int, nmm<V>()>{});
  if constexpr (S + 1 < NSTAGE) {
    constexpr int nx2 = (S + 2 < NSTAGE) ? C::NLX : 0, nw2 = (S + 2 < NSTAGE) ? C::NLW : 0;
    constexpr int n = (V == V_T5) ? nw2 : nx2 + nw2;
    asm volatile("s_waitcnt vmcnt(%c0) lgkmcnt(0)\n\ts_barrier" :: "i"(n) : "memory");
  } else {
    asm volatile("s_waitcnt lgkmcnt(0)" ::: "memory");
  }
  half1<V, S>(c, fa, fb, std::make_integer_sequence<int, nmm<V>()>{});
}
template <int V, int... S> DI void stages(Ctx& c, Frags& fa, Frags& fb, std::integer_sequence<int, S...>) { (stage<V, S>(c, fa, fb), ...); }

template <int V> struct LastOrder;
template <> struct LastOrder<V_T1>  { static constexpr int kk[10] = {0, 0, 0, 1, 1, 0, 1, 0, 1, 1}, m[10] = {0, 1, 2, 0, 1, 3, 2, 4, 3, 4}; };
template <> struct LastOrder<V_T5>  { static constexpr int kk[10] = {0, 0, 0, 1, 1, 0, 1, 0, 1, 1}, m[10] = {0, 1, 2, 0, 1, 3, 2, 4, 3, 4}; };
template <> struct LastOrder<V_SSM> { static constexpr int kk[8] = {0, 0, 1, 0, 1, 0, 1, 1}, m[8] = {0, 1, 0, 2, 1, 3, 2, 3}; };
template <int V, int I> DI void last_mm(Ctx& c, Frags& fa, Frags& fb) {
  constexpr int kk = LastOrder<V>::kk[I >> 2], m = LastOrder<V>::m[I >> 2], n = I & 3;
  mm<V, false, n * Cfg<V>::NX + m>(c, kk ? fb : fa);
  if constexpr (I < nreads<V>()) rd<V, NSTAGE - 1, 1, I>(c, fb);
  if constexpr (V != V_SSM && I % 3 == 1 && I / 3 < 7) side_ld<6 + I / 3>(*c.side);
}
template <int V, int S, int... I> DI void ldx_all(Ctx& c, std::integer_sequence<int, I...>) { (ldx<V, S, I>(c), ...); }
template <int V, int S, int... I> DI void ldw_all(Ctx& c, std::integer_sequence<int, I...>) { (ldw<V, S, I>(c), ...); }
template <int V, int... I> DI void rd_first(Ctx& c, Frags& f, std::integer_sequence<int, I...>) { (rd<V, 0, 0, I>(c, f), ...); }

template <int V> DI void prologue_loads(Ctx& c) {
  using C = Cfg<V>;
  ldx_all<V, 0>(c, std::make_integer_sequence<int, C::NLX>{}); ldw_all<V, 0>(c, std::make_integer_sequence<int, C::NLW>{});
  ldx_all<V, 1>(c, std::make_integer_sequence<int, C::NLX>{}); ldw_all<V, 1>(c, std::make_integer_sequence<int, C::NLW>{});
}
template <int V> constexpr int n_prologue() { return 2 * (Cfg<V>::NLX + Cfg<V>::NLW); }
template <int V> DI void mainloop(Ctx& c, Frags& fa, Frags& fb) {
  asm volatile("s_waitcnt vmcnt(%c0)\n\ts_barrier" :: "i"(Cfg<V>::NLX + Cfg<V>::NLW) : "memory");
  rd_first<V>(c, fa, std::make_integer_sequence<int, nreads<V>()>{});
  stages<V>(c, fa, fb, std::make_integer_sequence<int, NSTAGE - 1>{});
}

DI unsigned mt_token0(unsigned mt) { return 64u * (mt >> 2) + 32u * ((mt >> 1) & 1) + 8u * (mt & 1); }

DI f32x2 cfma(f32x2 a, f32x2 b, f32x2 c) {
  float r, i;
  asm("v_fma_f32 %0, %2, %4, %6\n\tv_fma_f32 %1, %2, %5, %7\n\tv_fma_f32 %0, -%3, %5, %0\n\tv_fma_f32 %1, %3, %4, %1"
      : "=&v"(r), "=&v"(i) : "v"(a[0]), "v"(a[1]), "v"(b[0]), "v"(b[1]), "v"(c[0]), "v"(c[1]));
  return (f32x2){r, i};
}
DI f32x2 cmul(f32x2 a, f32x2 b) {
  float r, i;
  asm("v_mul_f32 %0, %2, %4\n\tv_mul_f32 %1, %2, %5\n\tv_fma_f32 %0, -%3, %5, %0\n\tv_fma_f32 %1, %3, %4, %1"
      : "=&v"(r), "=&v"(i) : "v"(a[0]), "v"(a[1]), "v"(b[0]), "v"(b[1]));
  return (f32x2){r, i};
}
DI f32x2 cadd(f32x2 a, f32x2 b) {
  float r, i;
  asm("v_add_f32 %0, %2, %4\n\tv_add_f32 %1, %3, %5" : "=&v"(r), "=&v"(i) : "v"(a[0]), "v"(a[1]), "v"(b[0]), "v"(b[1]));
  return (f32x2){r, i};
}
struct Params { float lr, li, cr, ci; };
DI void sincos_f(const float x, float& sn, float& cs) {
  if (__builtin_expect(__any(fabsf(x) > 16384.f), 0)) { sincosf(x, &sn, &cs); return; }
  const float k = rintf(x * 0.6366197723675814f);
  float r = fmaf(k, -1.5707963705062866f, x); r = fmaf(k, 4.371138828673793e-08f, r); r = fmaf(k, 1.7151245100058819e-15f, r);
  const float z = r * r;
  const float s = fmaf(r * z, fmaf(z, fmaf(z, fmaf(z, 2.7557314297e-06f, -1.9841270114e-04f), 8.3333337680e-03f), -1.6666667163e-01f), r);
  const float c = fmaf(z * z, fmaf(z, fmaf(z, fmaf(z, -2.7557314297e-07f, 2.4801587642e-05f), -1.3888889225e-03f), 4.1666667908e-02f), fmaf(z, -0.5f, 1.f));
  const int n = (int)k;
  const float a = (n & 1) ? c : s, b = (n & 1) ? s : c;
  sn = (n & 2) ? -a : a; cs = ((n + 1) & 2) ? -b : b;
}
DI Params ssm_params(float lre, float lim, float logdt) {
  const float dt = rbf(expf(logdt));
  const float zr = dt * lre, zi = dt * lim;
  const float e = expf(zr); float s, co; sincos_f(zi, s, co);
  Params p; p.lr = e * co; p.li = e * s;
  const f32x2 num = cmul((f32x2){p.lr - 1.f, p.li}, (f32x2){lre, -lim}), dd = cmul((f32x2){lre, lim}, (f32x2){lre, -lim});
  p.cr = num[0] / dd[0]; p.ci = num[1] / dd[0];
  return p;
}
}

struct K1Args {
  const u16 *x, *lre, *lim, *bre, *bim, *cre, *cim, *logdt, *wg, *wu, *wd, *gw;
  char* ws;
};


extern "C" __global__ void zz_code_end();
DI void code_touch(const unsigned slot, const unsigned w, const int lane) {
  unsigned long long pc, end;
  asm volatile("s_getpc_b64 %0" : "=s"(pc));
  asm volatile("s_getpc_b64 s[98:99]\n\ts_add_u32 s98, s98, zz_code_end@rel32@lo+4\n\ts_addc_u32 s99, s99, zz_code_end@rel32@hi+12\n\ts_mov_b64 %0, s[98:99]" : "=s"(end) :: "scc", "s98", "s99");
  const unsigned long long base = pc & ~127ull;
  unsigned span = (unsigned)end - (unsigned)base; span = span < (1u << 17) ? span : 0;
  span = __builtin_amdgcn_readfirstlane(span);
  const u32x4 srd = make_srd((const void*)base, span);
  const unsigned voff = lane < 2 ? ((slot * 8 + w) * 2 + lane) * 128u : 0x40000000u;
  asm volatile("s_mov_b32 m0, %0\n\ts_nop 4\n\tbuffer_load_dword %1, %2, 0 offen lds" :: "s"(163840u - 256u), "v"(voff), "s"(srd) : "memory");
}

DI void side_setup(const K1Args& a, k1::Side& s, const unsigned j, const int lane, const unsigned ldsbase) {
  const bool isC = j < 800;
  const unsigned dl = lane & 15;
  s.m0 = ldsbase;
  s.srdX = make_srd(a.x, L_ * D_ * 2); s.srdG = make_srd(a.gw, D_ * 2);
  s.srdA = make_srd(isC ? a.cre : a.wd, isC ? D_ * P_ * 2 : F_ * D_ * 2); s.srdB = make_srd(isC ? a.cim : a.wd, isC ? D_ * P_ * 2 : F_ * D_ * 2);
  s.srdP[0] = make_srd(a.lre, P_ * 2); s.srdP[1] = make_srd(a.lim, P_ * 2); s.srdP[2] = make_srd(a.logdt, P_ * 2);
  s.v16 = lane * 16; s.v4 = lane * 4; s.vp = dl * 2;
  s.vw = isC ? (lane >> 1) * 1280u + (lane & 1) * 16u : (lane >> 2) * 1280u + (lane & 3) * 16u;
  s.sx[0] = j * 1280u; s.sx[1] = (j + 1408u) * 1280u;
  const unsigned cn = j / 20, np = j % 20, soC = np * 32u * 1280u + cn * 32u;
  const unsigned e0 = 2 * (j - 800), e1 = e0 + 1;
  const unsigned so0 = e0 < 1080 ? (e0 / 20) * 32u * 1280u + (e0 % 20) * 64u : 0x40000000u, so1 = e1 < 1080 ? (e1 / 20) * 32u * 1280u + (e1 % 20) * 64u : 0x40000000u;
  s.sw[0] = isC ? soC : so0;
  s.sw[1] = isC ? soC : so0 + 16u * 1280u;
  s.sw[2] = isC ? 0x40000000u : so1;
  s.sw[3] = isC ? 0x40000000u : so1 + 16u * 1280u;
  s.sp = isC ? cn * 32u : 0x40000000u;
}
namespace k1 {
template <int I> DI void side_ld(Side& s) {
  if constexpr (I < 4) glds(s.m0 + I * 1024u, s.vw, (I & 1) ? s.srdB : s.srdA, s.sw[I]);
  if constexpr (I >= 4 && I < 7) asm volatile("buffer_load_ushort %0, %1, %2, %3 offen" : "=v"(s.pr[I - 4]) : "v"(s.vp), "s"(s.srdP[I - 4]), "s"(s.sp) : "memory");
  if constexpr (I == 7) asm volatile("buffer_load_dwordx4 %0, %1, %2, 0 offen" : "=v"(s.ga) : "v"(s.v16), "s"(s.srdG) : "memory");
  if constexpr (I == 8) asm volatile("buffer_load_dword %0, %1, %2, 0 offen offset:1024" : "=v"(s.gb) : "v"(s.v4), "s"(s.srdG) : "memory");
  if constexpr (I == 9 || I == 11) asm volatile("buffer_load_dwordx4 %0, %1, %2, %3 offen" : "=v"(s.xa[(I - 9) / 2]) : "v"(s.v16), "s"(s.srdX), "s"(s.sx[(I - 9) / 2]) : "memory");
  if constexpr (I == 10 || I == 12) asm volatile("buffer_load_dword %0, %1, %2, %3 offen offset:1024" : "=v"(s.xb[(I - 10) / 2]) : "v"(s.v4), "s"(s.srdX), "s"(s.sx[(I - 10) / 2]) : "memory");
}
}
DI float dot2bf(const unsigned a, const unsigned b, const float c) { return __builtin_amdgcn_fdot2_f32_bf16(__builtin_bit_cast(bf16x2, a), __builtin_bit_cast(bf16x2, b), c, false); }
template <int CTRL, int ROWMASK> DI float dpp_add(float v) {
  return v + __uint_as_float(__builtin_amdgcn_update_dpp(0u, __float_as_uint(v), CTRL, ROWMASK, 0xf, true));
}
DI float wave_sum63(float v) {
  v = dpp_add<0x111, 0xf>(v); v = dpp_add<0x112, 0xf>(v); v = dpp_add<0x114, 0xf>(v); v = dpp_add<0x118, 0xf>(v);
  v = dpp_add<0x142, 0xa>(v); v = dpp_add<0x143, 0xc>(v);
  return v;
}
typedef __bf16 bf16x8 __attribute__((ext_vector_type(8)));
DI f32x4 mfma_sel(u32x4 a, s16x8 sel) { const f32x4 z = {0.f, 0.f, 0.f, 0.f}; return __builtin_amdgcn_mfma_f32_16x16x32_bf16(__builtin_bit_cast(bf16x8, a), __builtin_bit_cast(bf16x8, sel), z, 0, 0, 0); }
DI void make_sel(s16x8& S0, s16x8& S1, const int lane) {
  const unsigned tl = lane & 15, q = lane >> 4;
  const bool on0 = (q >> 1) == 0 && (tl >> 3) == (q & 1), on1 = (q >> 1) == 1 && (tl >> 3) == (q & 1);
#pragma unroll
  for (int j = 0; j < 8; ++j) { const bool e = (tl & 7) == (unsigned)j; S0[j] = (on0 && e) ? (short)0x3f80 : (short)0; S1[j] = (on1 && e) ? (short)0x3f80 : (short)0; }
}
DI void side_finish(const K1Args& a, k1::Side& s, LAS char* lds, const unsigned j, const int lane) {
  const __amdgpu_buffer_rsrc_t rW = __builtin_amdgcn_make_buffer_rsrc(a.ws + WS_WF, 0, WF_BYTES, 0x00020000);
  const __amdgpu_buffer_rsrc_t rG = __builtin_amdgcn_make_buffer_rsrc(a.ws + WS_GATE, 0, L_ * 4, 0x00020000);
  const unsigned q = lane >> 4;
  if (j < 800) {
    const unsigned cn = j / 20, np = j % 20;
    const k1::Params p = k1::ssm_params(bf_lo(s.pr[0]), bf_lo(s.pr[1]), bf_lo(s.pr[2]));
    float cr[4], ci[4];
#pragma unroll
    for (int r = 0; r < 4; ++r) { cr[r] = __shfl(2.f * p.cr, 4 * q + r); ci[r] = __shfl(2.f * p.ci, 4 * q + r); }
#pragma unroll
    for (int i = 0; i < 2; ++i) {
      const u32x2 re = *(LAS const u32x2*)(lds + s.m0 + (16 * i + (lane & 15)) * 32 + q * 8), im = *(LAS const u32x2*)(lds + s.m0 + 1024 + (16 * i + (lane & 15)) * 32 + q * 8);
      const float vr[4] = {bf_lo(re[0]), bf_hi(re[0]), bf_lo(re[1]), bf_hi(re[1])}, vi[4] = {bf_lo(im[0]), bf_hi(im[0]), bf_lo(im[1]), bf_hi(im[1])};
      float o0[4], o1[4];
#pragma unroll
      for (int r = 0; r < 4; ++r) { const f32x2 z = k1::cmul((f32x2){cr[r], ci[r]}, (f32x2){vr[r], vi[r]}); o0[r] = z[0]; o1[r] = -z[1]; }
      const u32x4 o = {cvtpk(o0[0], o0[1]), cvtpk(o0[2], o0[3]), cvtpk(o1[0], o1[1]), cvtpk(o1[2], o1[3])};
      const unsigned nt = 2 * np + i, dg = nt / 5, n = nt % 5;
      store16(o, rW, lane * 16 + ((dg * KSTEPS + cn) * 5 + n) * 1024u, WT);
    }
  } else {
    s16x8 S0, S1; make_sel(S0, S1, lane);
#pragma unroll
    for (int r = 0; r < 2; ++r) {
      const unsigned e = 2 * (j - 800) + r, ks = e / 20, np = e % 20;
      const u32x4 wa = *(LAS const u32x4*)(lds + s.m0 + 2048 * r + (lane & 15) * 64 + q * 16), wb = *(LAS const u32x4*)(lds + s.m0 + 2048 * r + 1024 + (lane & 15) * 64 + q * 16);
#pragma unroll
      for (int i = 0; i < 2; ++i) {
        const f32x4 da = mfma_sel(wa, i ? S1 : S0), db = mfma_sel(wb, i ? S1 : S0);
        const u32x4 o = {cvtpk(da[0], da[1]), cvtpk(da[2], da[3]), cvtpk(db[0], db[1]), cvtpk(db[2], db[3])};
        const unsigned nt = 2 * np + i, dg = nt / 5, n = nt % 5;
        store16(o, rW, e < 1080 ? lane * 16 + ((dg * KSTEPS + 40 + ks) * 5 + n) * 1024u : 0x40000000u, WT);
      }
    }
  }
  float sum[2] = {0.f, 0.f};
#pragma unroll
  for (int i = 0; i < 5; ++i) {
    const unsigned wv = i < 4 ? s.ga[i & 3] : s.gb, wl = wv & 0xffffu, wh = wv & 0xffff0000u;
#pragma unroll
    for (int r = 0; r < 2; ++r) {
      const unsigned xv = i < 4 ? s.xa[r][i & 3] : s.xb[r];
      sum[r] = dot2bf(cvtpk(dot2bf(xv, wl, 0.f), dot2bf(xv, wh, 0.f)), 0x3f803f80u, sum[r]);
    }
  }
#pragma unroll
  for (int r = 0; r < 2; ++r) sum[r] = wave_sum63(sum[r]);
  const float z0 = __uint_as_float(__builtin_amdgcn_readlane(__float_as_uint(sum[0]), 63)), z1 = __uint_as_float(__builtin_amdgcn_readlane(__float_as_uint(sum[1]), 63));
  const float z = lane == 0 ? z0 : z1;
  const float g = rbf(1.f / (1.f + expf(-rbf(z))));
  __builtin_amdgcn_raw_buffer_store_b32(__float_as_uint(g), rG, lane < 2 ? (j + 1408u * lane) * 4u : 0x40000000u, 0, WT);
}

namespace k1 {
struct Epi { __amdgpu_buffer_rsrc_t rA; unsigned so[5], v16; u32x4 o[5]; };
template <int M, int P> DI void epi_piece(Ctx& c, Epi& e) {
  constexpr int ft = P >> 1, r = 2 * (P & 1);
  if constexpr ((P & 1) == 0) asm volatile("" : "+v"(c.acc[M][2 * ft]), "+v"(c.acc[M][2 * ft + 1]));
  unsigned pk; float t0, t1, p0, p1;
  asm volatile("v_mul_f32 %1, 0xbfb8aa3b, %5\n\tv_mul_f32 %2, 0xbfb8aa3b, %6\n\tv_exp_f32 %1, %1\n\tv_exp_f32 %2, %2\n\t"
               "v_mul_f32 %3, %5, %7\n\tv_mul_f32 %4, %6, %8\n\tv_add_f32 %1, 1.0, %1\n\tv_add_f32 %2, 1.0, %2\n\t"
               "v_rcp_f32 %1, %1\n\tv_rcp_f32 %2, %2\n\tv_mul_f32 %3, %3, %1\n\tv_mul_f32 %4, %4, %2\n\tv_cvt_pk_bf16_f32 %0, %3, %4"
               : "=&v"(pk), "=&v"(t0), "=&v"(t1), "=&v"(p0), "=&v"(p1)
               : "v"(c.acc[M][2 * ft][r]), "v"(c.acc[M][2 * ft][r + 1]), "v"(c.acc[M][2 * ft + 1][r]), "v"(c.acc[M][2 * ft + 1][r + 1]));
  e.o[M][P] = pk;
  if constexpr (P == 3) store16(e.o[M], e.rA, e.v16 + e.so[M], WT);
}
template <int V, int I> DI void ffn_last_item(Ctx& c, Frags& fa, Frags& fb, Epi& e) {
  last_mm<V, I>(c, fa, fb);
  if constexpr (I >= 21 && I <= 24) epi_piece<0, I - 21>(c, e);
  if constexpr (I >= 25 && I <= 28) epi_piece<1, I - 25>(c, e);
  if constexpr (I >= 33 && I <= 36) epi_piece<2, I - 33>(c, e);
}
template <int V, int... I> DI void ffn_last(Ctx& c, Frags& fa, Frags& fb, Epi& e, std::integer_sequence<int, I...>) { (ffn_last_item<V, I>(c, fa, fb, e), ...); }
}
template <int V> DI void ffn_body(const K1Args& a, LAS char* lds, const unsigned w, const int lane, const unsigned tile, const unsigned grp, const unsigned sjob) {
  using namespace k1;
  using C = Cfg<V>;
  Ctx c; c.lds = lds; c.w = w;
  const unsigned wm = (V == V_T1) ? (w >> 2) : (w >> 1), wn = (V == V_T1) ? (w & 3) : (w & 1);
  const unsigned mt0 = tile * ((V == V_T1) ? 10u : 20u);
  const unsigned f0 = (V == V_T1) ? grp * 128u : 1664u;
  c.srdX = make_srd(a.x, L_ * D_ * 2); c.srdW0 = make_srd(a.wg, D_ * F_ * 2); c.srdW1 = make_srd(a.wu, D_ * F_ * 2);
  const unsigned th = w & 1, r8 = lane >> 3, c8 = lane & 7;
  c.vx = r8 * XROW + ((c8 ^ (4 * th + (r8 >> 1))) * 16);
#pragma unroll
  for (int i = 0; i < (V == V_T1 ? 2 : 5); ++i) { unsigned mt = mt0 + (w >> 1) + 4 * i; mt = mt < 128 ? mt : 127; c.sx[i] = (mt_token0(mt) + 16 * th) * XROW; }
  if constexpr (V == V_T1) {
    const unsigned p = 16 + (w >> 1), th3 = p & 1, r = 4 * (w & 1) + (r8 & 3);
    unsigned mt = mt0 + (p >> 1); mt = mt < 128 ? mt : 127;
    c.vx3 = r * XROW + ((c8 ^ (4 * th3 + (r >> 1))) * 16);
    c.sx3 = (mt_token0(mt) + 16 * th3) * XROW;
    const unsigned kr = lane >> 4, pos = lane & 15, sz = kr | ((w & 1) << 2);
    c.vw = kr * WROW + ((pos ^ (sz << 1)) * 16);
    c.sw = 8 * w * WROW + f0 * 2;
  } else {
    const unsigned sz = ((r8 >> 1) & 1) | ((w & 1) << 1);
    c.vw = r8 * WROW + ((c8 ^ (sz << 1)) * 16);
    c.sw = 8 * w * WROW + f0 * 2;
  }
  const unsigned tl = lane & 15, q = lane >> 4;
  c.rx = (wm * 80 + tl) * 128 + ((q ^ (tl >> 1)) * 16);
  {
    const unsigned qq = tl >> 2, p = lane & 3;
    if constexpr (V == V_T1) {
      const unsigned sz = qq | ((q & 1) << 2);
      c.rw0 = (8 * q + qq) * 256 + (((4 * wn + (p >> 1)) ^ (sz << 1)) * 16) + (p & 1) * 8;
    } else {
      const unsigned sz = ((qq >> 1) & 1) | ((q & 1) << 1);
      c.rw0 = (8 * q + qq) * 128 + (((4 * wn + (p >> 1)) ^ (sz << 1)) * 16) + (p & 1) * 8;
    }
    c.rw1 = c.rw0 ^ 32;
  }
  Side s; c.side = &s;
  prologue_loads<V>(c);
  side_setup(a, s, sjob, lane, (V == V_T1 ? 53248u : 0u) + w * 4096u);
  Frags fa, fb;
  mainloop<V>(c, fa, fb);
  Epi e; e.rA = __builtin_amdgcn_make_buffer_rsrc(a.ws + WS_AF, 0, AF_BYTES, 0x00020000);
  {
    const unsigned ks = 40 + ((V == V_T1) ? grp * 4 : 52) + wn;
#pragma unroll
    for (int m = 0; m < 5; ++m) { const unsigned mt = mt0 + wm * 5 + m; e.so[m] = mt < 128 ? (((mt >> 3) * KSTEPS + ks) * 8 + (mt & 7)) * 1024u : 0x40000000u; }
    e.v16 = lane * 16;
  }
  ffn_last<V>(c, fa, fb, e, std::make_integer_sequence<int, 40>{});
  asm volatile("s_nop 3" ::: "memory");
  epi_piece<3, 0>(c, e); epi_piece<3, 1>(c, e); epi_piece<3, 2>(c, e); epi_piece<3, 3>(c, e);
  epi_piece<4, 0>(c, e); epi_piece<4, 1>(c, e); epi_piece<4, 2>(c, e); epi_piece<4, 3>(c, e);
  asm volatile("s_waitcnt vmcnt(5)" : "+v"(s.xa[0]), "+v"(s.xa[1]), "+v"(s.xb[0]), "+v"(s.xb[1]), "+v"(s.ga), "+v"(s.gb), "+v"(s.pr[0]), "+v"(s.pr[1]), "+v"(s.pr[2]) :: "memory");
  side_finish(a, s, lds, sjob, lane);
}

namespace k1 {
struct Scan { f32x2 lam[2], e[2]; };
template <int M, int R> DI void scan_piece(Ctx& c, Scan& s) {
  if constexpr (R == 0) asm volatile("" : "+v"(c.acc[M][0]), "+v"(c.acc[M][1]), "+v"(c.acc[M][2]), "+v"(c.acc[M][3]));
#pragma unroll
  for (int cn = 0; cn < 2; ++cn) {
    const f32x2 in = {c.acc[M][2 * cn][R], c.acc[M][2 * cn + 1][R]};
    if constexpr (M == 0 && R == 0) s.e[cn] = in; else s.e[cn] = cfma(s.lam[cn], s.e[cn], in);
  }
  asm volatile("" : "+v"(s.e[0]), "+v"(s.e[1]));
}
template <int I> DI void ssm_last_item(Ctx& c, Frags& fa, Frags& fb, Scan& s) {
  last_mm<V_SSM, I>(c, fa, fb);
  if constexpr (I >= 19 && I <= 22) scan_piece<0, I - 19>(c, s);
  if constexpr (I >= 27 && I <= 30) scan_piece<1, I - 27>(c, s);
}
template <int... I> DI void ssm_last(Ctx& c, Frags& fa, Frags& fb, Scan& s, std::integer_sequence<int, I...>) { (ssm_last_item<I>(c, fa, fb, s), ...); }
}
template <int DUMMY> DI void ssm_body(const K1Args& a, LAS char* lds, const unsigned w, const int lane, const unsigned tile, const unsigned grp) {
  using namespace k1;
  constexpr int V = V_SSM;
  Ctx c; c.lds = lds; c.w = w;
  const unsigned wm = w >> 2, wn = w & 3;
  c.srdX = make_srd(a.x, L_ * D_ * 2); c.srdW0 = make_srd(a.bre, P_ * D_ * 2); c.srdW1 = make_srd(a.bim, P_ * D_ * 2);
  const unsigned th = w & 1, r8 = lane >> 3, c8 = lane & 7;
  const unsigned swz8 = (c8 ^ (4 * th + (r8 >> 1))) * 16;
  c.vx = (16 * (r8 >> 2) + (r8 & 3)) * XROW + swz8;
#pragma unroll
  for (int i = 0; i < 2; ++i) c.sx[i] = (tile * 128 + 64 * i + 4 * (w >> 1) + 32 * th) * XROW;
  c.vw = r8 * XROW + swz8;
  c.sw = (grp * 128 + 8 * w) * XROW;
  const unsigned tl = lane & 15, q = lane >> 4;
  c.rx = (wm * 64 + tl) * 128 + ((q ^ (tl >> 1)) * 16);
  c.rw0 = (wn * 32 + tl) * 128 + ((q ^ (tl >> 1)) * 16); c.rw1 = c.rw0;
  const unsigned ch_t = (w & 1) * 64 + lane;
  unsigned epoch = 0, pr0 = 0, pr1 = 0, pr2 = 0;
  const u32x4 srdXC = make_srd(a.ws + WS_XCH, 4096u);
  asm volatile("s_nop 4" ::: "memory");
  if (w < 2) {
    const u32x4 s0 = make_srd(a.lre, P_ * 2), s1 = make_srd(a.lim, P_ * 2), s2 = make_srd(a.logdt, P_ * 2);
    asm volatile("buffer_load_dword %0, %1, %2, %3 offen sc1" : "=v"(epoch) : "v"(0), "s"(srdXC), "s"(grp * 256u) : "memory");
    asm volatile("buffer_load_ushort %0, %1, %2, %3 offen" : "=v"(pr0) : "v"(ch_t * 2), "s"(s0), "s"(grp * 256u) : "memory");
    asm volatile("buffer_load_ushort %0, %1, %2, %3 offen" : "=v"(pr1) : "v"(ch_t * 2), "s"(s1), "s"(grp * 256u) : "memory");
    asm volatile("buffer_load_ushort %0, %1, %2, %3 offen" : "=v"(pr2) : "v"(ch_t * 2), "s"(s2), "s"(grp * 256u) : "memory");
  }

  prologue_loads<V>(c);
  if (w < 2) {
    asm volatile("s_waitcnt vmcnt(%c4)" : "+v"(epoch), "+v"(pr0), "+v"(pr1), "+v"(pr2) : "i"(n_prologue<V>()) : "memory");
    const Params p = ssm_params(bf_lo(pr0), bf_lo(pr1), bf_lo(pr2));
    f32x2 pw = {p.lr, p.li};
    *(LAS f32x2*)(lds + SS_PRM + ch_t * 8) = pw;
#pragma unroll
    for (int i = 0; i < 4; ++i) pw = cmul(pw, pw);
    *(LAS f32x2*)(lds + SS_POW + ch_t * 16) = pw;
#pragma unroll
    for (int i = 0; i < 3; ++i) pw = cmul(pw, pw);
    *(LAS f32x2*)(lds + SS_POW + ch_t * 16 + 8) = pw;
    asm volatile("s_waitcnt lgkmcnt(0)" ::: "memory");
    epoch += epoch == 0xffffffffu ? 2u : 1u;
  }
  Frags fa, fb;
  mainloop<V>(c, fa, fb);
  Scan sc;
  const unsigned seg = 4 * wm + q;
#pragma unroll
  for (int cn = 0; cn < 2; ++cn) sc.lam[cn] = *(LAS const f32x2*)(lds + SS_PRM + (32 * wn + 16 * cn + tl) * 8);
  ssm_last(c, fa, fb, sc, std::make_integer_sequence<int, 32>{});
  asm volatile("s_nop 3" ::: "memory");
  scan_piece<2, 0>(c, sc); scan_piece<2, 1>(c, sc); scan_piece<2, 2>(c, sc); scan_piece<2, 3>(c, sc);
  scan_piece<3, 0>(c, sc); scan_piece<3, 1>(c, sc); scan_piece<3, 2>(c, sc); scan_piece<3, 3>(c, sc);
  f32x2 lam[2] = {sc.lam[0], sc.lam[1]};
#pragma unroll
  for (int cn = 0; cn < 2; ++cn) *(LAS f32x2*)(lds + SS_E + (seg * 128 + 32 * wn + 16 * cn + tl) * 8) = sc.e[cn];
  asm volatile("s_waitcnt lgkmcnt(0)\n\ts_barrier" ::: "memory");
  s16x8 S0, S1;
  if (w >= 2) { make_sel(S0, S1, lane); asm volatile("" : "+v"(S0), "+v"(S1)); }
  if (w < 2) {
    const f32x2 l16 = *(LAS const f32x2*)(lds + SS_POW + ch_t * 16), l128 = *(LAS const f32x2*)(lds + SS_POW + ch_t * 16 + 8);
    f32x2 P[9]; P[0] = (f32x2){0.f, 0.f};
    P[1] = *(LAS const f32x2*)(lds + SS_E + ch_t * 8);
#pragma unroll
    for (int sg = 1; sg < 8; ++sg) P[sg + 1] = cfma(l16, P[sg], *(LAS const f32x2*)(lds + SS_E + (sg * 128 + ch_t) * 8));
    const __amdgpu_buffer_rsrc_t rX = __builtin_amdgcn_make_buffer_rsrc(a.ws + WS_XCH, 0, XCH_BYTES, 0x00020000);
    const unsigned gbase = 4096u + grp * 16u * 2048u;
    if (tile < 15) {
      const u32x4 gr = {__float_as_uint(P[8][0]), epoch, __float_as_uint(P[8][1]), epoch};
      store16(gr, rX, ch_t * 16 + gbase + tile * 2048u, 16);
    }
    make_sel(S0, S1, lane); asm volatile("" : "+v"(S0), "+v"(S1));
    f32x2 pw16[8]; pw16[1] = l16;
#pragma unroll
    for (int sg = 2; sg < 8; ++sg) pw16[sg] = cmul(pw16[sg - 1], l16);
    f32x2 carry = {0.f, 0.f};
    if (tile > 0) {
      const u32x4 srdg = make_srd(a.ws + WS_XCH + gbase, 16u * 2048u);
      u32x4 gk[15];
      for (unsigned spins = 0;; ++spins) {
#pragma unroll
        for (int j = 0; j < 15; ++j)
          asm volatile("buffer_load_dwordx4 %0, %1, %2, %3 offen sc1" : "=v"(gk[j]) : "v"(ch_t * 16), "s"(srdg), "s"((unsigned)j < tile ? (tile - 1 - j) * 2048u : 0x40000000u) : "memory");
        asm volatile("s_waitcnt vmcnt(0)" : "+v"(gk[0]), "+v"(gk[1]), "+v"(gk[2]), "+v"(gk[3]), "+v"(gk[4]), "+v"(gk[5]), "+v"(gk[6]), "+v"(gk[7]),
                     "+v"(gk[8]), "+v"(gk[9]), "+v"(gk[10]), "+v"(gk[11]), "+v"(gk[12]), "+v"(gk[13]), "+v"(gk[14]) :: "memory");
        unsigned cnt = 0;
#pragma unroll
        for (int j = 0; j < 15; ++j) cnt += (gk[j][1] == epoch) & (gk[j][3] == epoch);
        if (__all(cnt == tile) || spins > (1u << 20)) break;
        __builtin_amdgcn_s_sleep(1);
      }
#pragma unroll
      for (int j = 14; j >= 0; --j) carry = cfma(l128, carry, (f32x2){__uint_as_float(gk[j][0]), __uint_as_float(gk[j][2])});
    }
    *(LAS f32x2*)(lds + SS_X + ch_t * 8) = carry;
#pragma unroll
    for (int sg = 1; sg < 8; ++sg) *(LAS f32x2*)(lds + SS_X + (sg * 128 + ch_t) * 8) = cfma(pw16[sg], carry, P[sg]);
  }
  asm volatile("s_waitcnt lgkmcnt(0)\n\ts_barrier" ::: "memory");
  const __amdgpu_buffer_rsrc_t rA = __builtin_amdgcn_make_buffer_rsrc(a.ws + WS_AF, 0, AF_BYTES, 0x00020000);
  f32x2 h[2];
#pragma unroll
  for (int cn = 0; cn < 2; ++cn) h[cn] = *(LAS const f32x2*)(lds + SS_X + (seg * 128 + 32 * wn + 16 * cn + tl) * 8);
#pragma unroll
  for (int hh = 0; hh < 2; ++hh) {
    float hr[2][8], hi[2][8];
#pragma unroll
    for (int j = 0; j < 8; ++j)
#pragma unroll
      for (int cn = 0; cn < 2; ++cn) {
        const int i = 8 * hh + j;
        h[cn] = cfma(lam[cn], h[cn], (f32x2){c.acc[i >> 2][2 * cn][i & 3], c.acc[i >> 2][2 * cn + 1][i & 3]});
        hr[cn][j] = h[cn][0]; hi[cn][j] = h[cn][1];
      }
#pragma unroll
    for (int cn = 0; cn < 2; ++cn) {
      const unsigned ks = grp * 8 + wn * 2 + cn;
      const u32x4 ar = {cvtpk(hr[cn][0], hr[cn][1]), cvtpk(hr[cn][2], hr[cn][3]), cvtpk(hr[cn][4], hr[cn][5]), cvtpk(hr[cn][6], hr[cn][7])};
      const u32x4 ai = {cvtpk(hi[cn][0], hi[cn][1]), cvtpk(hi[cn][2], hi[cn][3]), cvtpk(hi[cn][4], hi[cn][5]), cvtpk(hi[cn][6], hi[cn][7])};
#pragma unroll
      for (int Q = 0; Q < 2; ++Q) {
        const f32x4 dr = mfma_sel(ar, Q ? S1 : S0), di = mfma_sel(ai, Q ? S1 : S0);
        const u32x4 o = {cvtpk(dr[0], dr[1]), cvtpk(dr[2], dr[3]), cvtpk(di[0], di[1]), cvtpk(di[2], di[3])};
        store16(o, rA, lane * 16 + ((tile * KSTEPS + ks) * 8 + 4 * wm + 2 * Q + hh) * 1024u, WT);
      }
    }
  }
  if (tile == 15 && w == 0 && lane == 0) {
    __hip_atomic_store((unsigned*)(a.ws + WS_XCH + grp * 256u), epoch, __ATOMIC_RELAXED, __HIP_MEMORY_SCOPE_AGENT);
  }
}

__global__ __launch_bounds__(512, 2) void k1_kernel(const K1Args a) {
  extern __shared__ __attribute__((aligned(16))) char lds_raw[];
  LAS char* lds = (LAS char*)lds_raw;
  const int lane = threadIdx.x & 63;
  const unsigned w = __builtin_amdgcn_readfirstlane(threadIdx.x >> 6);
  code_touch(blockIdx.x >> 3, w, lane);
  const unsigned xcd = blockIdx.x & 7, slot = blockIdx.x >> 3;
  const unsigned is = xcd * 10 + slot, fi = xcd * 22 + slot - 10;
  const unsigned type = slot < 10 ? k1::V_SSM : (fi < 169 ? k1::V_T1 : k1::V_T5);
  const unsigned grp = slot < 10 ? is >> 4 : fi / 13, tile = slot < 10 ? is & 15 : (fi < 169 ? fi % 13 : fi - 169);
  const unsigned sjob = fi * 8u + w;
  if (__builtin_expect(type == k1::V_T1, 1)) ffn_body<k1::V_T1>(a, lds, w, lane, tile, grp, sjob);
  else if (type == k1::V_SSM) ssm_body<0>(a, lds, w, lane, tile, grp);
  else ffn_body<k1::V_T5>(a, lds, w, lane, tile, 0, sjob);
}


namespace k2 {
constexpr int NSLOT = 3;
constexpr int SLOTR = 52;
constexpr int RED_W = 30720;
constexpr int XS_OFF = 4 * RED_W, XS_W = 6144, LDS_K2 = XS_OFF + 4 * XS_W;
constexpr int TP = 1088;

#define K2_ACLOB "a0","a1","a2","a3","a4","a5","a6","a7","a8","a9","a10","a11","a12","a13","a14","a15","a16","a17","a18","a19","a20","a21","a22","a23","a24","a25","a26","a27","a28","a29","a30","a31","a32","a33","a34","a35","a36","a37","a38","a39","a40","a41","a42","a43","a44","a45","a46","a47","a48","a49","a50","a51","a52","a53","a54","a55","a56","a57","a58","a59","a60","a61","a62","a63","a64","a65","a66","a67","a68","a69","a70","a71","a72","a73","a74","a75","a76","a77","a78","a79","a80","a81","a82","a83","a84","a85","a86","a87","a88","a89","a90","a91","a92","a93","a94","a95","a96","a97","a98","a99","a100","a101","a102","a103","a104","a105","a106","a107","a108","a109","a110","a111","a112","a113","a114","a115","a116","a117","a118","a119","a120","a121","a122","a123","a124","a125","a126","a127","a128","a129","a130","a131","a132","a133","a134","a135","a136","a137","a138","a139","a140","a141","a142","a143","a144","a145","a146","a147","a148","a149","a150","a151","a152","a153","a154","a155"

template <int ROLE> struct RC;
template <> struct RC<0> { static constexpr int NSTEP = 10, XSTEP = 1; };
template <> struct RC<1> { static constexpr int NSTEP = 14, XSTEP = 99; };

struct Ctx {
  u32x4 srdA, srdW, srdX, srdD, srdH;
  unsigned va[8];
  unsigned vw0, vw4;
  unsigned abase, wbase;
  bool full;
  unsigned xm0, tok0, xcol;
  u32x2 xv, dv;
  LAS char *xs, *ds, *red;
  unsigned w;
  f32x4 acc[8][5];
};

template <int ROLE, int STEP> DI void step_soff(Ctx& c, unsigned& sa, unsigned& sw) {
  sa = (c.abase + STEP) * 8192u;
  sw = (c.wbase + STEP) * 5120u;
  if constexpr (ROLE == 1 && STEP == RC<1>::NSTEP - 1) { sa = c.full ? sa : 0x40000000u; sw = c.full ? sw : 0x40000000u; }
}
template <int STEP, int I> DI void ld(Ctx& c, unsigned sa, unsigned sw) {
  constexpr int r = (STEP % NSLOT) * SLOTR + 4 * I;
  if constexpr (I < 8) asm volatile("buffer_load_dwordx4 a[%c0:%c1], %2, %3, %4 offen" :: "i"(r), "i"(r + 3), "v"(c.va[I]), "s"(c.srdA), "s"(sa) : "memory");
  else if constexpr (I < 12) asm volatile("buffer_load_dwordx4 a[%c0:%c1], %2, %3, %4 offen offset:%c5" :: "i"(r), "i"(r + 3), "v"(c.vw0), "s"(c.srdW), "s"(sw), "i"((I - 8) * 1024) : "memory");
  else asm volatile("buffer_load_dwordx4 a[%c0:%c1], %2, %3, %4 offen" :: "i"(r), "i"(r + 3), "v"(c.vw4), "s"(c.srdW), "s"(sw) : "memory");
}
template <int STEP, int M, int N> DI void mm(Ctx& c) {
  constexpr int base = (STEP % NSLOT) * SLOTR, ra = base + 4 * M, rw = base + 32 + 4 * N;
  if constexpr (STEP == 0) asm volatile("v_mfma_f32_16x16x32_bf16 %0, a[%c1:%c2], a[%c3:%c4], 0" : "=v"(c.acc[M][N]) : "i"(rw), "i"(rw + 3), "i"(ra), "i"(ra + 3) : "memory");
  else asm volatile("v_mfma_f32_16x16x32_bf16 %0, a[%c1:%c2], a[%c3:%c4], %0" : "+v"(c.acc[M][N]) : "i"(rw), "i"(rw + 3), "i"(ra), "i"(ra + 3) : "memory");
}
template <int N> DI void waitv() { asm volatile("s_waitcnt vmcnt(%c0)" :: "i"(N) : "memory"); }

template <int ROLE> constexpr int n_extra() { return ROLE == 0 ? 6 : 0; }
template <int ROLE> DI void extra_loads(Ctx& c) {
  if constexpr (ROLE == 0) {
    unsigned lane = c.vw0 >> 4; asm volatile("" : "+v"(lane));
#pragma unroll
    for (int i = 0; i < 5; ++i) {
      const unsigned p = i * 64 + lane, r = p / 10, ch = p % 10;
      const unsigned off = (c.tok0 + 8 * (r >> 4) + 16 * ((r >> 3) & 1) + (r & 7)) * 1280u + c.xcol + ch * 16u;
      asm volatile("s_mov_b32 m0, %0\n\ts_nop 0\n\tbuffer_load_dwordx4 %1, %2, 0 offen lds" :: "s"(c.xm0 + i * 1024u), "v"(off), "s"(c.srdX) : "memory");
    }
    const unsigned dof = lane < 10 ? c.xcol + lane * 16u : 0x40000000u;
    asm volatile("s_mov_b32 m0, %0\n\ts_nop 0\n\tbuffer_load_dwordx4 %1, %2, 0 offen lds" :: "s"(c.xm0 + 5120u), "v"(dof), "s"(c.srdD) : "memory");
  }
}
template <int J, int N> DI void skip_read(Ctx& c) { c.xv = *(LAS const u32x2*)(c.xs + J * 2560 + N * 32); c.dv = *(LAS const u32x2*)(c.ds + N * 32); }
template <int J, int N> DI void skip_term(Ctx& c) {
  const u32x2 xv = c.xv, dv = c.dv;
  asm volatile("" : "+v"(c.acc[J][N]));
  float v0 = c.acc[J][N][0], v1 = c.acc[J][N][1], v2 = c.acc[J][N][2], v3 = c.acc[J][N][3];
  unsigned t0, t1, t2, t3;
  asm volatile("v_and_b32 %4, 0xffff, %10\n\tv_and_b32 %5, 0xffff0000, %10\n\tv_and_b32 %6, 0xffff, %11\n\tv_and_b32 %7, 0xffff0000, %11\n\t"
               "v_dot2c_f32_bf16 %0, %8, %4\n\tv_dot2c_f32_bf16 %1, %8, %5\n\tv_dot2c_f32_bf16 %2, %9, %6\n\tv_dot2c_f32_bf16 %3, %9, %7"
               : "+v"(v0), "+v"(v1), "+v"(v2), "+v"(v3), "=&v"(t0), "=&v"(t1), "=&v"(t2), "=&v"(t3)
               : "v"(xv[0]), "v"(xv[1]), "v"(dv[0]), "v"(dv[1]));
  c.acc[J][N] = (f32x4){v0, v1, v2, v3};
  asm volatile("s_nop 2" : "+v"(c.acc[J][N]));
}
template <int ROLE, int STEP, int I> DI void item(Ctx& c, unsigned sa, unsigned sw) {
  mm<STEP, I / 5, I % 5>(c);
  if constexpr (STEP + NSLOT - 1 < RC<ROLE>::NSTEP && I % 3 == 1 && I / 3 < 13) ld<STEP + NSLOT - 1, I / 3>(c, sa, sw);
  if constexpr (ROLE == 0 && STEP >= 4 && STEP <= 7) {
    constexpr int t0 = STEP < 6 ? 3 * (STEP - 4) : 6 + 2 * (STEP - 6), nt = STEP < 6 ? 3 : 2;
    if constexpr (I >= 15 && (I - 15) % 7 == 0 && (I - 15) / 7 < nt) skip_read<(t0 + (I - 15) / 7) / 5, (t0 + (I - 15) / 7) % 5>(c);
    if constexpr (I >= 20 && (I - 20) % 7 == 0 && (I - 20) / 7 < nt) skip_term<(t0 + (I - 20) / 7) / 5, (t0 + (I - 20) / 7) % 5>(c);
  }
}
template <int ROLE, int STEP, int... I> DI void step_items(Ctx& c, unsigned sa, unsigned sw, std::integer_sequence<int, I...>) { (item<ROLE, STEP, I>(c, sa, sw), ...); }
template <int ROLE, int STEP> DI void step(Ctx& c) {
  constexpr int NSTEP = RC<ROLE>::NSTEP, XSTEP = RC<ROLE>::XSTEP, LS = STEP + NSLOT - 1;
  unsigned sa = 0, sw = 0;
  if constexpr (LS < NSTEP) step_soff<ROLE, LS>(c, sa, sw);
  constexpr int last_issued = (STEP + NSLOT - 2 < NSTEP - 1) ? STEP + NSLOT - 2 : NSTEP - 1;
  constexpr int extra = (STEP > XSTEP && STEP <= XSTEP + NSLOT - 1) ? n_extra<ROLE>() : 0;
  waitv<13 * (last_issued - STEP) + extra>();
  step_items<ROLE, STEP>(c, sa, sw, std::make_integer_sequence<int, 40>{});
  if constexpr (STEP == XSTEP) extra_loads<ROLE>(c);
}
template <int STEP, int... I> DI void pro_items(Ctx& c, unsigned sa, unsigned sw, std::integer_sequence<int, I...>) { (ld<STEP, I>(c, sa, sw), ...); }
template <int ROLE, int STEP> DI void prologue_loads(Ctx& c) {
  unsigned sa, sw; step_soff<ROLE, STEP>(c, sa, sw);
  pro_items<STEP>(c, sa, sw, std::integer_sequence<int, 0, 8, 9, 10, 11, 12, 1, 2, 3, 4, 5, 6, 7>{});
}
template <int ROLE, int... S> DI void steps(Ctx& c, std::integer_sequence<int, S...>) { (step<ROLE, S>(c), ...); }
template <int I, int N> DI void red_write(Ctx& c) {
  asm volatile("" : "+v"(c.acc[I][N]));
  *(LAS f32x4*)(c.red + ((c.w + (I >> 1)) & 3) * RED_W + ((I >> 1) - 1) * 10240 + (I & 1) * 5120 + N * 1024) = c.acc[I][N];
}
template <int ROLE, int I> DI void tail_item(Ctx& c) {
  constexpr int g = I / 10, k = I % 10, m = (g + 2) & 7, NSTEP = RC<ROLE>::NSTEP;
  mm<NSTEP - 2 + k / 5, m, k % 5>(c);
  if constexpr (g >= 1 && g <= 6 && k >= 4 && k <= 8) red_write<g + 1, k - 4>(c);
}
template <int ROLE, int... I> DI void tail_items(Ctx& c, std::integer_sequence<int, I...>) { (tail_item<ROLE, I>(c), ...); }
template <int ROLE> DI void tail(Ctx& c) {
  waitv<0>();
  tail_items<ROLE>(c, std::make_integer_sequence<int, 80>{});
}
}

struct K2Args {
  const unsigned *AF, *WF; const float* gate; const u16 *x, *dskip; float* out; char* ho;
};

DI void partner_loads(u32x4 (&pt)[2][5], const unsigned poff, const u32x4 srdP) {
#pragma unroll
  for (int j = 0; j < 2; ++j)
#pragma unroll
    for (int n = 0; n < 5; ++n) asm volatile("buffer_load_dwordx4 %0, %1, %2, %3 offen sc1" : "=v"(pt[j][n]) : "v"(poff), "s"(srdP), "s"((j * 5 + n) * 1024u) : "memory");
}
DI bool partner_valid(const u32x4 (&pt)[2][5], const unsigned tlo, const unsigned thi) {
  unsigned bad = 0;
#pragma unroll
  for (int j = 0; j < 2; ++j)
#pragma unroll
    for (int n = 0; n < 5; ++n) bad |= ((pt[j][n][0] ^ tlo) | (pt[j][n][1] ^ thi) | (pt[j][n][2] ^ tlo) | (pt[j][n][3] ^ thi)) & 0xfffu;
  return __all(bad == 0);
}
template <int ROLE> DI void k2_body(const K2Args& a, LAS char* lds, const int w, const int lane, const unsigned tt, const unsigned dg) {
  using namespace k2;
  const int tl = lane & 15, q = lane >> 4;
  const unsigned pair = tt * 8 + dg;
  Ctx c;
  c.srdA = make_srd(a.AF, AF_BYTES); c.srdW = make_srd(a.WF, WF_BYTES); c.srdH = make_srd(a.ho, HO_WORDS);
  c.srdX = make_srd(a.x, 2048 * 640 * 2); c.srdD = make_srd(a.dskip, 640 * 2);
  const u32x4 srdG = make_srd(a.gate, 2048 * 4);
#pragma unroll
  for (int i = 0; i < 8; ++i) c.va[i] = lane * 16 + ((i + 2 * w) & 7) * 1024;
  c.vw0 = lane * 16; c.vw4 = lane * 16 + 4096;
  const unsigned k0 = ROLE == 0 ? 10 * w : 40 + (w < 2 ? 14 * w : 28 + 13 * (w - 2));
  c.abase = tt * KSTEPS + k0; c.wbase = dg * KSTEPS + k0; c.full = w < 2;
  c.tok0 = 128 * tt + 64 * (w >> 1) + 32 * (w & 1); c.xcol = 160u * dg; c.xm0 = XS_OFF + w * XS_W;
  c.xs = lds + c.xm0 + tl * 160 + q * 8; c.ds = lds + c.xm0 + 5120 + q * 8; c.red = lds + lane * 16; c.w = w;
  unsigned graw, epoch;
  asm volatile("s_nop 4\n\tbuffer_load_dword %0, %1, %2, %3 offen" : "=v"(graw) : "v"(lane * 4), "s"(srdG), "s"(c.tok0 * 4u) : "memory");
  asm volatile("buffer_load_dword %0, %1, %2, %3 offen sc1" : "=v"(epoch) : "v"(0), "s"(c.srdH), "s"(pair * 256u) : "memory");
  prologue_loads<ROLE, 0>(c); prologue_loads<ROLE, 1>(c);
  static_assert(NSLOT == 3, "prologue");
  asm volatile("s_waitcnt vmcnt(26)" : "+v"(graw), "+v"(epoch) :: "memory", K2_ACLOB);
  float g[2];
#pragma unroll
  for (int j = 0; j < 2; ++j) g[j] = __int_as_float(__builtin_amdgcn_ds_bpermute((8 * j + 16 * (tl >> 3) + (tl & 7)) * 4, (int)graw));
  epoch += 1;
  steps<ROLE>(c, std::make_integer_sequence<int, RC<ROLE>::NSTEP - 2>{});
  unsigned prd[5], pst[5];
  if constexpr (ROLE == 1) {
    unsigned ln = lane; asm volatile("" : "+v"(ln));
#pragma unroll
    for (int i = 0; i < 5; ++i) {
      const unsigned p = i * 64 + ln, r = p / 20, ch = p % 20;
      prd[i] = (ch >> 2) * TP + (ch & 3) * 272 + r * 16;
      pst[i] = (c.tok0 + 16 * (r >> 3) + (r & 7)) * 2560u + 320u * dg + ch * 16u;
      asm volatile("" : "+v"(prd[i]), "+v"(pst[i]));
    }
  }
  tail<ROLE>(c);
  asm volatile("s_nop 7" ::: "memory");
#pragma unroll
  for (int m = 0; m < 2; ++m) asm volatile("" : "+v"(c.acc[m][0]), "+v"(c.acc[m][1]), "+v"(c.acc[m][2]), "+v"(c.acc[m][3]), "+v"(c.acc[m][4]));
  const __amdgpu_buffer_rsrc_t rP = __builtin_amdgcn_make_buffer_rsrc(a.ho + HO_WORDS, 0, HO_TILES, 0x00020000);
  const unsigned poff = (pair * 8 + 2 * w) * 5120u + lane * 16u;
  asm volatile("s_waitcnt lgkmcnt(0)\n\ts_barrier" ::: "memory");
  u32x4 ptile[2][5];
  const u32x4 srdP = make_srd(a.ho + HO_WORDS, HO_TILES);
  const unsigned tlo = epoch & 0xfffu, thi = (epoch >> 12) & 0xfffu;
  if constexpr (ROLE == 1) partner_loads(ptile, poff, srdP);
  f32x4 v[2][5];
  const LAS char* src = lds + w * RED_W + lane * 16;
#pragma unroll
  for (int j = 0; j < 2; ++j) {
#pragma unroll
    for (int n = 0; n < 5; ++n) v[j][n] = (c.acc[j][n] + *(const LAS f32x4*)(src + (j * 5 + n) * 1024)) + (*(const LAS f32x4*)(src + 10240 + (j * 5 + n) * 1024) + *(const LAS f32x4*)(src + 20480 + (j * 5 + n) * 1024));
  }
  if constexpr (ROLE == 0) {
#pragma unroll
    for (int j = 0; j < 2; ++j)
#pragma unroll
      for (int n = 0; n < 5; ++n) {
        const u32x4 b = __builtin_bit_cast(u32x4, v[j][n] * g[j]);
        const u32x4 o = {(b[0] & ~0xfffu) | tlo, (b[1] & ~0xfffu) | thi, (b[2] & ~0xfffu) | tlo, (b[3] & ~0xfffu) | thi};
        store16(o, rP, poff + (j * 5 + n) * 1024u, 16);
      }
  } else {
#pragma unroll
    for (int j = 0; j < 2; ++j) asm volatile("" : "+v"(v[j][0]), "+v"(v[j][1]), "+v"(v[j][2]), "+v"(v[j][3]), "+v"(v[j][4]));
    asm volatile("s_waitcnt vmcnt(0) lgkmcnt(0)" : "+v"(ptile[0][0]), "+v"(ptile[0][1]), "+v"(ptile[0][2]), "+v"(ptile[0][3]), "+v"(ptile[0][4]),
                 "+v"(ptile[1][0]), "+v"(ptile[1][1]), "+v"(ptile[1][2]), "+v"(ptile[1][3]), "+v"(ptile[1][4]) :: "memory");
    for (unsigned spins = 0; !partner_valid(ptile, tlo, thi) && spins < (1u << 20); ++spins) {
      __builtin_amdgcn_s_sleep(1);
      asm volatile("buffer_load_dwordx4 %0, %10, %11, 0 offen sc1\n\tbuffer_load_dwordx4 %1, %10, %11, 0 offen offset:1024 sc1\n\t"
                   "buffer_load_dwordx4 %2, %10, %11, 0 offen offset:2048 sc1\n\tbuffer_load_dwordx4 %3, %10, %11, 0 offen offset:3072 sc1\n\t"
                   "buffer_load_dwordx4 %4, %10, %11, %12 offen sc1\n\tbuffer_load_dwordx4 %5, %10, %11, %12 offen offset:1024 sc1\n\t"
                   "buffer_load_dwordx4 %6, %10, %11, %12 offen offset:2048 sc1\n\tbuffer_load_dwordx4 %7, %10, %11, %12 offen offset:3072 sc1\n\t"
                   "buffer_load_dwordx4 %8, %10, %11, %13 offen sc1\n\tbuffer_load_dwordx4 %9, %10, %11, %13 offen offset:1024 sc1\n\ts_waitcnt vmcnt(0)"
                   : "=&v"(ptile[0][0]), "=&v"(ptile[0][1]), "=&v"(ptile[0][2]), "=&v"(ptile[0][3]), "=&v"(ptile[0][4]),
                     "=&v"(ptile[1][0]), "=&v"(ptile[1][1]), "=&v"(ptile[1][2]), "=&v"(ptile[1][3]), "=&v"(ptile[1][4])
                   : "v"(poff), "s"(srdP), "s"(4096u), "s"(8192u) : "memory");
    }
#pragma unroll
    for (int j = 0; j < 2; ++j)
#pragma unroll
      for (int n = 0; n < 5; ++n) ptile[j][n] &= ~0xfffu;
    LAS char* tr = lds + w * RED_W;
#pragma unroll
    for (int j = 0; j < 2; ++j) {
      const float g1 = rbf(1.f - g[j]);
#pragma unroll
      for (int n = 0; n < 5; ++n) *(LAS f32x4*)(tr + (j * 5 + n) * TP + q * 272 + tl * 16) = __builtin_bit_cast(f32x4, ptile[j][n]) + g1 * v[j][n];
    }
    const __amdgpu_buffer_rsrc_t rO = __builtin_amdgcn_make_buffer_rsrc(a.out, 0, 2048 * 640 * 4, 0x00020000);
#pragma unroll
    for (int j = 0; j < 2; ++j)
#pragma unroll
      for (int i = 0; i < 5; ++i) {
        const f32x4 o = *(const LAS f32x4*)(tr + j * 5 * TP + prd[i]);
        store16(__builtin_bit_cast(u32x4, o), rO, pst[i] + j * 8 * 2560u, WT);
      }
    asm volatile("s_barrier" ::: "memory");
    if (w == 0 && lane == 0) {
      const __amdgpu_buffer_rsrc_t rH = __builtin_amdgcn_make_buffer_rsrc(a.ho, 0, HO_WORDS, 0x00020000);
      __builtin_amdgcn_raw_buffer_store_b32(epoch, rH, pair * 256u, 0, 16);
    }
  }
}

__global__ __launch_bounds__(256, 1) void k2_kernel(const K2Args a) {
  extern __shared__ __attribute__((aligned(16))) char k2_lds[];
  LAS char* lds = (LAS char*)k2_lds;
  const int lane = threadIdx.x & 63;
  const int w = __builtin_amdgcn_readfirstlane(threadIdx.x >> 6);
  const unsigned xcd = blockIdx.x & 7, idx = blockIdx.x >> 3;
  const unsigned tt = (xcd >> 1) * 4 + (idx >> 3), dg = (xcd & 1) * 4 + ((idx >> 1) & 3);
  if (idx & 1) k2_body<1>(a, lds, w, lane, tt, dg); else k2_body<0>(a, lds, w, lane, tt, dg);
}

extern "C" __global__ void zz_code_end() {}
extern "C" void kernel_launch(void* const* d_in, const int* in_sizes, int n_in, void* d_out, int out_size, void* d_ws, size_t ws_size, hipStream_t stream) {
  K1Args a;
  a.x = (const u16*)d_in[0]; a.lre = (const u16*)d_in[1]; a.lim = (const u16*)d_in[2]; a.bre = (const u16*)d_in[3]; a.bim = (const u16*)d_in[4];
  a.cre = (const u16*)d_in[5]; a.cim = (const u16*)d_in[6]; a.logdt = (const u16*)d_in[7];
  a.wg = (const u16*)d_in[10]; a.wu = (const u16*)d_in[11]; a.wd = (const u16*)d_in[12]; a.gw = (const u16*)d_in[13];
  a.ws = (char*)d_ws;
  (void)hipFuncSetAttribute((const void*)k1_kernel, hipFuncAttributeMaxDynamicSharedMemorySize, k1::LDS_BYTES);
  k1_kernel<<<256, 512, k1::LDS_BYTES, stream>>>(a);
  char* ws = (char*)d_ws;
  K2Args b;
  b.AF = (const unsigned*)(ws + WS_AF); b.WF = (const unsigned*)(ws + WS_WF); b.gate = (const float*)(ws + WS_GATE);
  b.x = (const u16*)d_in[0]; b.dskip = (const u16*)d_in[8]; b.out = (float*)d_out; b.ho = ws + WS_HO;
  (void)hipFuncSetAttribute((const void*)k2_kernel, hipFuncAttributeMaxDynamicSharedMemorySize, k2::LDS_K2);
  k2_kernel<<<256, 256, k2::LDS_K2, stream>>>(b);
}
```
